```python
import torch

def _softmax(x, axis=-1):
    return torch.softmax(x, dim=axis)
def _gelu(x):
    return torch.nn.functional.gelu(x, approximate="tanh")
def _silu(x):
    return torch.nn.functional.silu(x)
def _sigmoid(x):
    return torch.sigmoid(x)
def _layernorm(x, axis=-1, eps=1e-5):
    m = x.mean(dim=axis, keepdim=True)
    v = x.var(dim=axis, keepdim=True, unbiased=False)
    return (x - m) / torch.sqrt(v + eps)
def _scatter_add(tgt, idx, src, axis=0):
    return tgt.clone().index_add_(axis, idx, src)
def _segment_sum(src, seg, n):
    out = torch.zeros((n,) + src.shape[1:], dtype=src.dtype)
    return out.index_add_(0, seg, src)
def _pad(x, pw):
    flat = tuple(v for pair in reversed(pw) for v in pair)
    return torch.nn.functional.pad(x, flat)

kernel_name = 'interleave_layers__ssm_conv_h3__dense_activation_silu__prompt'

def setup_inputs():

    _w = (lambda a: (lambda t: t.to(torch.bfloat16) if t.is_floating_point() else t)(torch.as_tensor(a)))
    def _A_setup():
        B, S, D = 1, 2048, 640
        ks = 4
        return {
            "q": _w((lambda *s: torch.randn(*s, dtype=torch.bfloat16) * 0.05)(B, S, D)),
            "k": _w((lambda *s: torch.randn(*s, dtype=torch.bfloat16) * 0.2)(B, S, D)),
            "v": _w((lambda *s: torch.randn(*s, dtype=torch.bfloat16) * 0.2)(B, S, D)),
            "shift_k": _w((lambda *s: torch.randn(*s, dtype=torch.bfloat16) * 0.2)(D, ks) * 0.5),
            "ssm_k": _w((lambda *s: torch.randn(*s, dtype=torch.bfloat16) * 0.8)(D, S) * (1.0 / S ** 0.5)),
            "ssm_D": _w((lambda *s: torch.randn(*s, dtype=torch.bfloat16) * 0.2)(D)),
        }
    _A_in = _A_setup()

    def _B_setup():
        B, S, D, F = 1, 2048, 640, 1728
        return {
            "x": _w((lambda *s: torch.randn(*s, dtype=torch.bfloat16) * 0.05)(B, S, D)),
            "w1": _w((lambda *s: torch.randn(*s, dtype=torch.bfloat16) * 0.8)(D, F) * (D ** -0.5)),
            "w2": _w((lambda *s: torch.randn(*s, dtype=torch.bfloat16) * 0.2)(F, D) * (F ** -0.5)),
        }
    _B_in = _B_setup()

    return {
        "A_q": _A_in["q"],
        "A_k": _A_in["k"],
        "A_v": _A_in["v"],
        "A_shift_k": _A_in["shift_k"],
        "A_ssm_k": _A_in["ssm_k"],
        "A_ssm_D": _A_in["ssm_D"],
        "B_x": _B_in["x"],
        "B_w1": _B_in["w1"],
        "B_w2": _B_in["w2"],
    }

def reference(A_q, A_k, A_v, A_shift_k, A_ssm_k, A_ssm_D, B_x, B_w1, B_w2):
    def _A_ref(q, k, v, shift_k, ssm_k, ssm_D):
        B, S, D = q.shape
        ks = shift_k.shape[-1]
        k_pad = _pad(k, ((0, 0), (ks - 1, 0), (0, 0)))
        k_shift = torch.zeros_like(k)
        for t in range(ks):
            k_shift = k_shift + k_pad[:, t:t + S, :] * shift_k[:, ks - 1 - t][None, None, :]
        kv = k_shift * v
        L = 2 * S
        kv_pad = _pad(kv, ((0, 0), (0, S), (0, 0)))
        kf_pad = _pad(ssm_k, ((0, 0), (0, S)))
        kv_f = (lambda x, axes=(-2,-1): torch.fft.rfft2(x.float(), dim=axes))(kv_pad[:, None, :, :], axes=(1, 2))
        kf_f = (lambda x, axes=(-2,-1): torch.fft.rfft2(x.float(), dim=axes))(kf_pad[:, None, :], axes=(1, 2))
        kf_f = (lambda x, *d: x.permute(*d))(kf_f, 1, 2, 0)
        y_f = kv_f * kf_f[None, :, :, :]
        y = (lambda x, s, axes=(-2,-1): torch.fft.irfft2(x, s=s, dim=axes).to(torch.bfloat16))(y_f, (1, L), axes=(1, 2))[:, 0, :S, :]
        y = y + kv * ssm_D[None, None, :]
        return q * y

    def _B_ref(x, w1, w2):
        h = torch.einsum("bsd,df->bsf", x, w1)
        h = _silu(h)
        out = torch.einsum("bsf,fd->bsd", h, w2)
        return out

    x = A_q
    x = x + _A_ref(_layernorm(x, axis=-1), A_k, A_v, A_shift_k, A_ssm_k, A_ssm_D)
    x = x + _B_ref(_layernorm(x, axis=-1), B_w1, B_w2)
    x = x + _A_ref(_layernorm(x, axis=-1), A_k, A_v, A_shift_k, A_ssm_k, A_ssm_D)
    x = x + _B_ref(_layernorm(x, axis=-1), B_w1, B_w2)
    return x
```

```cpp
#include <hip/hip_runtime.h>
#include <hip/hip_bf16.h>
#include <stdint.h>

typedef __attribute__((ext_vector_type(8))) short bf16x8;
typedef __attribute__((ext_vector_type(4))) float f32x4;
typedef __attribute__((ext_vector_type(2))) float f32x2;
typedef __attribute__((ext_vector_type(4))) unsigned int u32x4;
typedef __attribute__((ext_vector_type(2))) unsigned int u32x2;
typedef __attribute__((ext_vector_type(2))) __bf16 bf2_t;

constexpr int S_ = 2048, D_ = 640, F_ = 1728;
constexpr int KS1 = D_ / 32;
constexpr int KS2 = F_ / 32;
constexpr int NB1 = F_ / 16;
constexpr int DB_ = D_ / 16;

constexpr size_t SZ_ACT = (size_t)S_ * D_ * 2;
constexpr size_t OFF_URT = 0;
constexpr size_t OFF_X0T = OFF_URT + SZ_ACT;
constexpr size_t OFF_Q0T = OFF_X0T + SZ_ACT;
constexpr size_t OFF_X1T = OFF_Q0T + SZ_ACT;
constexpr size_t OFF_YT  = OFF_X1T + SZ_ACT;
constexpr size_t OFF_X2T = OFF_YT + SZ_ACT;
constexpr size_t OFF_X3T = OFF_X2T + SZ_ACT;
constexpr size_t OFF_HT  = OFF_X3T + SZ_ACT;
constexpr size_t SZ_HT   = (size_t)S_ * F_ * 2;
constexpr size_t OFF_W1T = OFF_HT + SZ_HT;
constexpr size_t SZ_W    = (size_t)D_ * F_ * 2;
constexpr size_t OFF_W2T = OFF_W1T + SZ_W;
constexpr size_t OFF_CS  = OFF_W2T + SZ_W;

__device__ __forceinline__ float bflo(unsigned x) { return __uint_as_float(x << 16); }
__device__ __forceinline__ float bfhi(unsigned x) { return __uint_as_float(x & 0xffff0000u); }
__device__ __forceinline__ unsigned pk2(float lo, float hi) {
    f32x2 v = {lo, hi};
    bf2_t r = __builtin_convertvector(v, bf2_t);
    return __builtin_bit_cast(unsigned, r);
}
__device__ __forceinline__ float rbf(float x) { return bflo(pk2(x, 0.f)); }

__global__ __launch_bounds__(320) void k0_prep(const unsigned* __restrict__ q, const unsigned* __restrict__ k,
                                               const unsigned* __restrict__ v, const u32x4* __restrict__ shk,
                                               char* __restrict__ ws) {
    __shared__ float part[8][160];
    __shared__ float stat[8][2];
    const int b = blockIdx.x, t = threadIdx.x;
    const int s0 = b * 8;
    const int g = t % 160, rh = t / 160;
    u32x2 xq[4];
#pragma unroll
    for (int i = 0; i < 4; ++i)
        xq[i] = *(const u32x2*)(q + (size_t)(s0 + 4 * rh + i) * (D_ / 2) + 2 * g);
    unsigned kr[11], vr[8];
#pragma unroll
    for (int i = 0; i < 11; ++i) {
        int s = s0 - 3 + i;
        kr[i] = (s >= 0) ? k[(size_t)s * (D_ / 2) + t] : 0u;
    }
#pragma unroll
    for (int i = 0; i < 8; ++i) vr[i] = v[(size_t)(s0 + i) * (D_ / 2) + t];
    const u32x4 w = shk[t];
    {
        float wa[4] = {bflo(w.x), bfhi(w.x), bflo(w.y), bfhi(w.y)};
        float wb[4] = {bflo(w.z), bfhi(w.z), bflo(w.w), bfhi(w.w)};
        float ua[8], ub[8];
#pragma unroll
        for (int i = 0; i < 8; ++i) {
            float a = rbf(bflo(kr[i]) * wa[3]);
            a = rbf(a + rbf(bflo(kr[i + 1]) * wa[2]));
            a = rbf(a + rbf(bflo(kr[i + 2]) * wa[1]));
            a = rbf(a + rbf(bflo(kr[i + 3]) * wa[0]));
            ua[i] = a * bflo(vr[i]);
            float c = rbf(bfhi(kr[i]) * wb[3]);
            c = rbf(c + rbf(bfhi(kr[i + 1]) * wb[2]));
            c = rbf(c + rbf(bfhi(kr[i + 2]) * wb[1]));
            c = rbf(c + rbf(bfhi(kr[i + 3]) * wb[0]));
            ub[i] = c * bfhi(vr[i]);
        }
        u32x4 oa = {pk2(ua[7], ua[6]), pk2(ua[5], ua[4]), pk2(ua[3], ua[2]), pk2(ua[1], ua[0])};
        u32x4 ob = {pk2(ub[7], ub[6]), pk2(ub[5], ub[4]), pk2(ub[3], ub[2]), pk2(ub[1], ub[0])};
        const int d = 2 * t;
        u32x4* dst = (u32x4*)(ws + OFF_URT) + ((size_t)(d >> 2) * 256 + (255 - b)) * 4 + (d & 3);
        dst[0] = oa;
        dst[1] = ob;
    }
    float xv[4][4];
#pragma unroll
    for (int i = 0; i < 4; ++i) {
        xv[i][0] = bflo(xq[i].x); xv[i][1] = bfhi(xq[i].x); xv[i][2] = bflo(xq[i].y); xv[i][3] = bfhi(xq[i].y);
        part[4 * rh + i][g] = (xv[i][0] + xv[i][1]) + (xv[i][2] + xv[i][3]);
    }
    __syncthreads();
    if (t < 256) {
        int r = t >> 5, l = t & 31;
        float s = 0.f;
#pragma unroll
        for (int i = 0; i < 5; ++i) s += part[r][l + 32 * i];
#pragma unroll
        for (int o = 16; o; o >>= 1) s += __shfl_xor(s, o, 32);
        if (l == 0) stat[r][0] = s * (1.f / D_);
    }
    __syncthreads();
#pragma unroll
    for (int i = 0; i < 4; ++i) {
        float m = stat[4 * rh + i][0];
        float a = xv[i][0] - m, bb = xv[i][1] - m, c = xv[i][2] - m, d = xv[i][3] - m;
        part[4 * rh + i][g] = (a * a + bb * bb) + (c * c + d * d);
    }
    __syncthreads();
    if (t < 256) {
        int r = t >> 5, l = t & 31;
        float s = 0.f;
#pragma unroll
        for (int i = 0; i < 5; ++i) s += part[r][l + 32 * i];
#pragma unroll
        for (int o = 16; o; o >>= 1) s += __shfl_xor(s, o, 32);
        if (l == 0) stat[r][1] = s * (1.f / D_);
    }
    __syncthreads();
    {
        const int d0 = 4 * g;
        const int ks = d0 >> 5, half = (d0 >> 4) & 1, qq = (d0 >> 2) & 3;
#pragma unroll
        for (int i = 0; i < 4; ++i) {
            const int s = s0 + 4 * rh + i;
            float m = rbf(stat[4 * rh + i][0]);
            float vv = rbf(stat[4 * rh + i][1]);
            float dn = rbf(sqrtf(rbf(vv + 1e-5f)));
            float rinv = 1.f / dn;
            float n0 = rbf(xv[i][0] - m) * rinv, n1 = rbf(xv[i][1] - m) * rinv;
            float n2 = rbf(xv[i][2] - m) * rinv, n3 = rbf(xv[i][3] - m) * rinv;
            size_t off = (((size_t)(s >> 4) * KS1 + ks) * 64 + qq * 16 + (s & 15)) * 16 + half * 8;
            *(u32x2*)(ws + OFF_X0T + off) = xq[i];
            u32x2 o = {pk2(n0, n1), pk2(n2, n3)};
            *(u32x2*)(ws + OFF_Q0T + off) = o;
        }
    }
}

constexpr int HC_STRIDE = 4160;
constexpr int CH_H = 4 * HC_STRIDE;
constexpr int UR_BYTES = 4608;
constexpr int CH_BYTES = CH_H + UR_BYTES;
constexpr int K1_RED_OFF = 4 * CH_BYTES;
constexpr int K1_LDS = K1_RED_OFF + 8 * 8192;

__device__ __forceinline__ void retile_block(const unsigned* __restrict__ W, int ncols, int nks, int cg, int u0, int u1,
                                             char* __restrict__ dstT, float* __restrict__ cs, float* lds) {
    const int t = threadIdx.x, lane = t & 63, wv = t >> 6;
    const int cp = lane & 31, hh = lane >> 5;
    const int n0 = cg * 64 + 2 * cp;
    float sa = 0.f, sb = 0.f;
    unsigned val[5][8];
#pragma unroll
    for (int i = 0; i < 5; ++i) {
        int u = u0 + wv * 10 + 2 * i + hh;
        int uu = u < u1 ? u : u1 - 1;
        int ks = uu >> 2, q = uu & 3;
#pragma unroll
        for (int j = 0; j < 8; ++j) {
            int kk = 32 * ks + (j >> 2) * 16 + 4 * q + (j & 3);
            val[i][j] = W[(size_t)kk * (ncols / 2) + (n0 >> 1)];
        }
    }
#pragma unroll
    for (int i = 0; i < 5; ++i) {
        int u = u0 + wv * 10 + 2 * i + hh;
        if (u < u1) {
            int ks = u >> 2, q = u & 3;
            u32x4 lo, hi;
            lo.x = __builtin_amdgcn_perm(val[i][1], val[i][0], 0x05040100u);
            lo.y = __builtin_amdgcn_perm(val[i][3], val[i][2], 0x05040100u);
            lo.z = __builtin_amdgcn_perm(val[i][5], val[i][4], 0x05040100u);
            lo.w = __builtin_amdgcn_perm(val[i][7], val[i][6], 0x05040100u);
            hi.x = __builtin_amdgcn_perm(val[i][1], val[i][0], 0x07060302u);
            hi.y = __builtin_amdgcn_perm(val[i][3], val[i][2], 0x07060302u);
            hi.z = __builtin_amdgcn_perm(val[i][5], val[i][4], 0x07060302u);
            hi.w = __builtin_amdgcn_perm(val[i][7], val[i][6], 0x07060302u);
            int nb = n0 >> 4, r = n0 & 15;
            u32x4* dst = (u32x4*)(dstT + (((size_t)nb * nks + ks) * 64 + q * 16 + r) * 16);
            dst[0] = lo;
            dst[1] = hi;
#pragma unroll
            for (int j = 0; j < 8; ++j) { sa += bflo(val[i][j]); sb += bfhi(val[i][j]); }
        }
    }
    if (cs) {
        lds[(wv * 2 + hh) * 64 + 2 * cp] = sa;
        lds[(wv * 2 + hh) * 64 + 2 * cp + 1] = sb;
        __syncthreads();
        if (t < 64) {
            float s = 0.f;
#pragma unroll
            for (int i = 0; i < 16; ++i) s += lds[i * 64 + t];
            cs[cg * 64 + t] = s;
        }
    }
}

__global__ __launch_bounds__(512) void k1_conv(const unsigned short* __restrict__ ssmk, const unsigned short* __restrict__ ssmD,
                                               const unsigned* __restrict__ w1, const unsigned* __restrict__ w2,
                                               char* __restrict__ ws) {
    extern __shared__ __attribute__((aligned(16))) char smem[];
    const int b = blockIdx.x, t = threadIdx.x;
    if (b >= 160) {
        int r = b - 160;
        if (r < 27) {
            retile_block(w1, F_, KS1, r, 0, 80, ws + OFF_W1T, (float*)(ws + OFF_CS), (float*)smem);
        } else {
            r -= 27;
            int cg = r / 3, part = r % 3;
            int u0 = part * 80, u1 = (u0 + 80 < 216) ? u0 + 80 : 216;
            retile_block(w2, D_, KS2, cg, u0, u1, ws + OFF_W2T, nullptr, (float*)smem);
        }
        return;
    }
    const int d0 = 4 * b;
    const int lane = t & 63, wv = __builtin_amdgcn_readfirstlane(t >> 6);
    const int eks = d0 >> 5, ehalf = (d0 >> 4) & 1, eq = (d0 >> 2) & 3;
    u32x2 x0r[4], q0r[4];
#pragma unroll
    for (int i = 0; i < 4; ++i) {
        int s = t + 512 * i;
        size_t off = (((size_t)(s >> 4) * KS1 + eks) * 64 + eq * 16 + (s & 15)) * 16 + ehalf * 8;
        x0r[i] = *(const u32x2*)(ws + OFF_X0T + off);
        q0r[i] = *(const u32x2*)(ws + OFF_Q0T + off);
    }
    const u32x2 dsk = *(const u32x2*)(ssmD + d0);
    {
        const u32x4* src = (const u32x4*)(ws + OFF_URT) + (size_t)b * 1024;
        u32x4 a0 = src[t], a1 = src[t + 512];
        u32x4 ha[3]; u32x2 hb[3];
#pragma unroll
        for (int e = 0; e < 3; ++e) {
            int it = t + 512 * e;
            int ch = it / 258, m = it % 258 - 2;
            ha[e] = (u32x4){0u, 0u, 0u, 0u}; hb[e] = (u32x2){0u, 0u};
            if (it < 1032) {
                const unsigned short* hp = ssmk + (size_t)(d0 + ch) * S_;
                if (m >= 0) ha[e] = *(const u32x4*)(hp + 8 * m);
                if (m >= -1 && m < 255) hb[e] = *(const u32x2*)(hp + 8 * m + 8);
            }
        }
        {
            int i0 = t, i1 = t + 512;
            *(u32x4*)(smem + (i0 & 3) * CH_BYTES + CH_H + (i0 >> 2) * 16) = a0;
            *(u32x4*)(smem + (i1 & 3) * CH_BYTES + CH_H + (i1 >> 2) * 16) = a1;
            if (t < 128) *(u32x4*)(smem + (t & 3) * CH_BYTES + CH_H + 4096 + (t >> 2) * 16) = (u32x4){0u, 0u, 0u, 0u};
        }
#pragma unroll
        for (int e = 0; e < 3; ++e) {
            int it = t + 512 * e;
            if (it < 1032) {
                int ch = it / 258, mp = it % 258;
                char* base = smem + ch * CH_BYTES + mp * 16;
                u32x4 A = ha[e]; u32x2 B = hb[e];
                u32x4 c1 = {__builtin_amdgcn_alignbit(A.y, A.x, 16), __builtin_amdgcn_alignbit(A.z, A.y, 16),
                            __builtin_amdgcn_alignbit(A.w, A.z, 16), __builtin_amdgcn_alignbit(B.x, A.w, 16)};
                u32x4 c2 = {A.y, A.z, A.w, B.x};
                u32x4 c3 = {c1.y, c1.z, c1.w, __builtin_amdgcn_alignbit(B.y, B.x, 16)};
                *(u32x4*)(base) = A;
                *(u32x4*)(base + HC_STRIDE) = c1;
                *(u32x4*)(base + 2 * HC_STRIDE) = c2;
                *(u32x4*)(base + 3 * HC_STRIDE) = c3;
            }
        }
    }
    __syncthreads();
    {
        const int ch = wv & 3, bh = wv >> 2;
        const int jr = lane & 15, q = lane >> 4;
        const char* abase = smem + ch * CH_BYTES + ((jr + 1) & 3) * HC_STRIDE + 16 * q + 2 * ((jr + 1) & ~3) + 64 * (4 * bh);
        const char* bbase = smem + ch * CH_BYTES + CH_H + 2 * (2032 - 16 * jr + 8 * q) + 64 * (4 * bh);
        f32x4 acc[8];
#pragma unroll
        for (int c = 0; c < 8; ++c) acc[c] = (f32x4){0.f, 0.f, 0.f, 0.f};
#pragma unroll
        for (int bb = 0; bb < 4; ++bb) {
            bf16x8 A[8], B[8];
#pragma unroll
            for (int m = 0; m < 8; ++m) B[m] = *(const bf16x8*)(bbase + 64 * bb - 512 * m);
#pragma unroll
            for (int a = 0; a < 8; ++a) {
                u32x2 lo = *(const u32x2*)(abase + 64 * bb + 512 * a);
                u32x2 hi = *(const u32x2*)(abase + 64 * bb + 512 * a + 8);
                u32x4 x = {lo.x, lo.y, hi.x, hi.y};
                A[a] = __builtin_bit_cast(bf16x8, x);
            }
#pragma unroll
            for (int a = 0; a < 8; ++a)
#pragma unroll
                for (int m = 0; m < 8 - a; ++m)
                    acc[a + m] = __builtin_amdgcn_mfma_f32_16x16x32_bf16(A[a], B[m], acc[a + m], 0, 0, 0);
        }
        float* red = (float*)(smem + K1_RED_OFF) + (ch * 2 + bh) * 2048;
#pragma unroll
        for (int c = 0; c < 8; ++c) *(f32x4*)(red + 256 * c + 16 * jr + 4 * q) = acc[c];
    }
    __syncthreads();
    {
        const float* red = (const float*)(smem + K1_RED_OFF);
        float dk[4] = {bflo(dsk.x), bfhi(dsk.x), bflo(dsk.y), bfhi(dsk.y)};
#pragma unroll
        for (int i = 0; i < 4; ++i) {
            int s = t + 512 * i;
            float xs[4] = {bflo(x0r[i].x), bfhi(x0r[i].x), bflo(x0r[i].y), bfhi(x0r[i].y)};
            float qs[4] = {bflo(q0r[i].x), bfhi(q0r[i].x), bflo(q0r[i].y), bfhi(q0r[i].y)};
            float yo[4], xo[4];
#pragma unroll
            for (int c = 0; c < 4; ++c) {
                float y = rbf(red[(c * 2) * 2048 + s] + red[(c * 2 + 1) * 2048 + s]);
                unsigned short uu = *(const unsigned short*)(smem + c * CH_BYTES + CH_H + 2 * (2047 - s));
                float u = bflo(uu);
                y = rbf(y + rbf(u * dk[c]));
                yo[c] = y;
                xo[c] = xs[c] + rbf(qs[c] * y);
            }
            size_t off = (((size_t)(s >> 4) * KS1 + eks) * 64 + eq * 16 + (s & 15)) * 16 + ehalf * 8;
            u32x2 oy = {pk2(yo[0], yo[1]), pk2(yo[2], yo[3])};
            u32x2 ox = {pk2(xo[0], xo[1]), pk2(xo[2], xo[3])};
            *(u32x2*)(ws + OFF_YT + off) = oy;
            *(u32x2*)(ws + OFF_X1T + off) = ox;
        }
    }
}

constexpr int G1_LDS = KS1 * 7 * 1024;

#define GLOAD_X4(dst, voff, sbase, imm) \
    asm volatile("global_load_dwordx4 %0, %1, %2 offset:%3" : "=v"(dst) : "v"(voff), "s"(sbase), "i"(imm) : "memory")

__global__ __launch_bounds__(256, 1) void k3_gemm1(const char* __restrict__ XT, const char* __restrict__ W1T,
                                                   const float* __restrict__ CS, char* __restrict__ HT) {
    extern __shared__ __attribute__((aligned(16))) char smem[];
    const int b = blockIdx.x;
    const int xcd = b & 7, idx = b >> 3;
    const int rp = 2 * xcd + (idx >> 4), ct = idx & 15;
    const int lane = threadIdx.x & 63;
    const int wv = __builtin_amdgcn_readfirstlane(threadIdx.x >> 6);
    const int rb0 = rp * 8 + 2 * wv;
    const char* xbase = XT + (size_t)rb0 * (KS1 * 1024);
    bf16x8 xf[2][KS1];
    unsigned voff = lane * 16;
#define ISSUE_CHUNK(c)                                                                                          \
    {                                                                                                           \
        unsigned v0 = voff + (c) * 4096;                                                                        \
        unsigned v1 = voff + KS1 * 1024 + (c) * 4096;                                                           \
        GLOAD_X4(xf[0][4 * (c) + 0], v0, xbase, 0);                                                             \
        GLOAD_X4(xf[1][4 * (c) + 0], v1, xbase, 0);                                                             \
        GLOAD_X4(xf[0][4 * (c) + 1], v0, xbase, 1024);                                                          \
        GLOAD_X4(xf[1][4 * (c) + 1], v1, xbase, 1024);                                                          \
        GLOAD_X4(xf[0][4 * (c) + 2], v0, xbase, 2048);                                                          \
        GLOAD_X4(xf[1][4 * (c) + 2], v1, xbase, 2048);                                                          \
        GLOAD_X4(xf[0][4 * (c) + 3], v0, xbase, 3072);                                                          \
        GLOAD_X4(xf[1][4 * (c) + 3], v1, xbase, 3072);                                                          \
        _Pragma("unroll") for (int i = 0; i < 7; ++i) {                                                         \
            int nb = 7 * ct + i; nb = nb < NB1 ? nb : NB1 - 1;                                                  \
            int ks = 4 * (c) + wv;                                                                              \
            __builtin_amdgcn_global_load_lds((const unsigned*)(W1T + ((size_t)nb * KS1 + ks) * 1024 + lane * 16), \
                                             (unsigned*)(smem + (ks * 7 + i) * 1024), 16, 0, 0);                \
        }                                                                                                       \
    }
    ISSUE_CHUNK(0) ISSUE_CHUNK(1) ISSUE_CHUNK(2) ISSUE_CHUNK(3)

    f32x4 acc[2][7];
#pragma unroll
    for (int i = 0; i < 7; ++i) { acc[0][i] = (f32x4){0.f, 0.f, 0.f, 0.f}; acc[1][i] = (f32x4){0.f, 0.f, 0.f, 0.f}; }
    float sx[2] = {0.f, 0.f}, sxx[2] = {0.f, 0.f};

#define WAIT_CHUNK(c, n)                                                                                       \
    asm volatile("s_waitcnt vmcnt(" #n ")"                                                                      \
                 : "+v"(xf[0][4 * (c)]), "+v"(xf[0][4 * (c) + 1]), "+v"(xf[0][4 * (c) + 2]), "+v"(xf[0][4 * (c) + 3]), \
                   "+v"(xf[1][4 * (c)]), "+v"(xf[1][4 * (c) + 1]), "+v"(xf[1][4 * (c) + 2]), "+v"(xf[1][4 * (c) + 3])  \
                 :: "memory");                                                                                  \
    __builtin_amdgcn_s_barrier();                                                                               \
    asm volatile("" ::: "memory");

#define COMPUTE_CHUNK(c)                                                                                        \
    _Pragma("unroll") for (int kk = 0; kk < 4; ++kk) {                                                          \
        const int ks = 4 * (c) + kk;                                                                            \
        bf16x8 wf[7];                                                                                           \
        _Pragma("unroll") for (int i = 0; i < 7; ++i) wf[i] = *(const bf16x8*)(smem + (ks * 7 + i) * 1024 + lane * 16); \
        _Pragma("unroll") for (int i = 0; i < 7; ++i) {                                                         \
            acc[0][i] = __builtin_amdgcn_mfma_f32_16x16x32_bf16(wf[i], xf[0][ks], acc[0][i], 0, 0, 0);          \
            acc[1][i] = __builtin_amdgcn_mfma_f32_16x16x32_bf16(wf[i], xf[1][ks], acc[1][i], 0, 0, 0);          \
        }                                                                                                       \
        _Pragma("unroll") for (int r = 0; r < 2; ++r) {                                                         \
            u32x4 xx = __builtin_bit_cast(u32x4, xf[r][ks]);                                                    \
            _Pragma("unroll") for (int e = 0; e < 4; ++e) {                                                     \
                unsigned xe = xx[e];                                                                            \
                bf2_t p = __builtin_bit_cast(bf2_t, xe);                                                        \
                bf2_t one = __builtin_bit_cast(bf2_t, 0x3f803f80u);                                             \
                sx[r] = __builtin_amdgcn_fdot2_f32_bf16(p, one, sx[r], false);                                  \
                sxx[r] = __builtin_amdgcn_fdot2_f32_bf16(p, p, sxx[r], false);                                  \
            }                                                                                                   \
        }                                                                                                       \
    }

    WAIT_CHUNK(0, 45)
    ISSUE_CHUNK(4)
    COMPUTE_CHUNK(0)
    WAIT_CHUNK(1, 45)
    COMPUTE_CHUNK(1)
    WAIT_CHUNK(2, 30)
    COMPUTE_CHUNK(2)
    WAIT_CHUNK(3, 15)
    COMPUTE_CHUNK(3)
    WAIT_CHUNK(4, 0)
    COMPUTE_CHUNK(4)

    const int q = lane >> 4;
    f32x4 csv[7];
#pragma unroll
    for (int i = 0; i < 7; ++i) {
        int nb = 7 * ct + i; nb = nb < NB1 ? nb : NB1 - 1;
        csv[i] = *(const f32x4*)(CS + nb * 16 + 4 * q);
    }
#pragma unroll
    for (int r = 0; r < 2; ++r) {
        float a = sx[r], bq = sxx[r];
        a += __shfl_xor(a, 16); bq += __shfl_xor(bq, 16);
        a += __shfl_xor(a, 32); bq += __shfl_xor(bq, 32);
        float mean = a * (1.f / D_);
        float var = fmaxf(bq * (1.f / D_) - mean * mean, 0.f);
        float m = rbf(mean);
        float dn = rbf(sqrtf(rbf(rbf(var) + 1e-5f)));
        float rinv = 1.f / dn;
        char* hrow = HT + ((size_t)(rb0 + r) * KS2 * 64 + lane) * 16;
#pragma unroll
        for (int i = 0; i < 7; ++i) {
            int nb = 7 * ct + i;
            float o[4];
#pragma unroll
            for (int e = 0; e < 4; ++e) {
                float h = rbf((acc[r][i][e] - m * csv[i][e]) * rinv);
                o[e] = h / (1.f + __expf(-h));
            }
            u32x2 st = {pk2(o[0], o[1]), pk2(o[2], o[3])};
            if (nb < NB1) *(u32x2*)(hrow + (size_t)(nb >> 1) * 1024 + (nb & 1) * 8) = st;
        }
    }
}

constexpr int G2_P = 5;
template <bool FINAL>
__global__ __launch_bounds__(256, 1) void k4_gemm2(const char* __restrict__ HT, const char* __restrict__ W2T,
                                                   const char* __restrict__ XIN, char* __restrict__ XOUT) {
    __shared__ __attribute__((aligned(16))) f32x4 red[4][4][5][64];
    const int b = blockIdx.x;
    const int xcd = b & 7, idx = b >> 3;
    const int rq = 4 * xcd + (idx >> 3), cu = idx & 7;
    const int lane = threadIdx.x & 63;
    const int wv = __builtin_amdgcn_readfirstlane(threadIdx.x >> 6);
    const int k0 = (wv < 2) ? 14 * wv : 28 + 13 * (wv - 2);
    const int n = (wv < 2) ? 14 : 13;
    const char* hb = HT + ((size_t)(rq * 4) * KS2 + k0) * 1024 + lane * 16;
    const char* wb = W2T + ((size_t)(cu * 5) * KS2 + k0) * 1024 + lane * 16;
    bf16x8 A[G2_P][5], B[G2_P][4];
    auto load = [&](int st, int i) {
        int ii = i < n ? i : n - 1;
#pragma unroll
        for (int j = 0; j < 5; ++j) A[st][j] = *(const bf16x8*)(wb + ((size_t)j * KS2 + ii) * 1024);
#pragma unroll
        for (int j = 0; j < 4; ++j) B[st][j] = *(const bf16x8*)(hb + ((size_t)j * KS2 + ii) * 1024);
    };
#pragma unroll
    for (int p = 0; p < G2_P; ++p) load(p, p);
    const int q = lane >> 4, r = lane & 15;
    const int rb = rq * 4 + wv;
    u32x2 xin[5];
#pragma unroll
    for (int j = 0; j < 5; ++j) {
        int db = 5 * cu + j;
        xin[j] = *(const u32x2*)(XIN + (((size_t)rb * KS1 + (db >> 1)) * 64 + lane) * 16 + (db & 1) * 8);
    }
    f32x4 acc[5][4];
#pragma unroll
    for (int j = 0; j < 5; ++j)
#pragma unroll
        for (int i = 0; i < 4; ++i) acc[j][i] = (f32x4){0.f, 0.f, 0.f, 0.f};
#pragma unroll
    for (int i = 0; i < 14; ++i) {
        const int st = i % G2_P;
        if (i < 13 || i < n) {
#pragma unroll
            for (int j = 0; j < 5; ++j)
#pragma unroll
                for (int c = 0; c < 4; ++c)
                    acc[j][c] = __builtin_amdgcn_mfma_f32_16x16x32_bf16(A[st][j], B[st][c], acc[j][c], 0, 0, 0);
        }
        if (i + G2_P < 14) load(st, i + G2_P);
    }
#pragma unroll
    for (int c = 0; c < 4; ++c)
        if (c != wv) {
#pragma unroll
            for (int j = 0; j < 5; ++j) red[wv][c][j][lane] = acc[j][c];
        }
    __syncthreads();
    f32x4 o[5];
#pragma unroll
    for (int j = 0; j < 5; ++j) {
        f32x4 own = (wv == 0) ? acc[j][0] : (wv == 1) ? acc[j][1] : (wv == 2) ? acc[j][2] : acc[j][3];
        f32x4 s = (f32x4){0.f, 0.f, 0.f, 0.f};
#pragma unroll
        for (int w2 = 0; w2 < 4; ++w2) {
            f32x4 p = (w2 == wv) ? own : red[w2][wv][j][lane];
            s += p;
        }
        o[j] = s;
    }
#pragma unroll
    for (int j = 0; j < 5; ++j) {
        int db = 5 * cu + j;
        float x0 = bflo(xin[j].x) + rbf(o[j][0]);
        float x1 = bfhi(xin[j].x) + rbf(o[j][1]);
        float x2 = bflo(xin[j].y) + rbf(o[j][2]);
        float x3 = bfhi(xin[j].y) + rbf(o[j][3]);
        u32x2 st = {pk2(x0, x1), pk2(x2, x3)};
        if (FINAL) {
            *(u32x2*)(XOUT + ((size_t)(rb * 16 + r) * D_ + db * 16 + 4 * q) * 2) = st;
        } else {
            *(u32x2*)(XOUT + (((size_t)rb * KS1 + (db >> 1)) * 64 + lane) * 16 + (db & 1) * 8) = st;
        }
    }
}

__global__ __launch_bounds__(64) void k5_mix(const u32x4* __restrict__ X2T, const u32x4* __restrict__ YT, u32x4* __restrict__ X3T) {
    const int rb = blockIdx.x, lane = threadIdx.x;
    u32x4 x[KS1], y[KS1];
#pragma unroll
    for (int ks = 0; ks < KS1; ++ks) x[ks] = X2T[((size_t)rb * KS1 + ks) * 64 + lane];
#pragma unroll
    for (int ks = 0; ks < KS1; ++ks) y[ks] = YT[((size_t)rb * KS1 + ks) * 64 + lane];
    float s = 0.f;
#pragma unroll
    for (int ks = 0; ks < KS1; ++ks)
#pragma unroll
        for (int e = 0; e < 4; ++e) s += bflo(x[ks][e]) + bfhi(x[ks][e]);
    s += __shfl_xor(s, 16); s += __shfl_xor(s, 32);
    float mean = s * (1.f / D_);
    float vs = 0.f;
#pragma unroll
    for (int ks = 0; ks < KS1; ++ks)
#pragma unroll
        for (int e = 0; e < 4; ++e) {
            float a = bflo(x[ks][e]) - mean, c = bfhi(x[ks][e]) - mean;
            vs += a * a + c * c;
        }
    vs += __shfl_xor(vs, 16); vs += __shfl_xor(vs, 32);
    float m = rbf(mean);
    float dn = rbf(sqrtf(rbf(rbf(vs * (1.f / D_)) + 1e-5f)));
    float rinv = 1.f / dn;
#pragma unroll
    for (int ks = 0; ks < KS1; ++ks) {
        u32x4 o;
#pragma unroll
        for (int e = 0; e < 4; ++e) {
            float a = bflo(x[ks][e]), c = bfhi(x[ks][e]);
            float na = rbf(rbf(a - m) * rinv), nc = rbf(rbf(c - m) * rinv);
            o[e] = pk2(a + rbf(na * bflo(y[ks][e])), c + rbf(nc * bfhi(y[ks][e])));
        }
        X3T[((size_t)rb * KS1 + ks) * 64 + lane] = o;
    }
}

extern "C" void kernel_launch(void* const* d_in, const int* in_sizes, int n_in,
                              void* d_out, int out_size, void* d_ws, size_t ws_size,
                              hipStream_t stream) {
    char* ws = (char*)d_ws;
    hipFuncSetAttribute((const void*)k1_conv, hipFuncAttributeMaxDynamicSharedMemorySize, K1_LDS);
    hipFuncSetAttribute((const void*)k3_gemm1, hipFuncAttributeMaxDynamicSharedMemorySize, G1_LDS);
    k0_prep<<<256, 320, 0, stream>>>((const unsigned*)d_in[0], (const unsigned*)d_in[1], (const unsigned*)d_in[2],
                                     (const u32x4*)d_in[3], ws);
    k1_conv<<<160 + 27 + 30, 512, K1_LDS, stream>>>((const unsigned short*)d_in[4], (const unsigned short*)d_in[5],
                                                    (const unsigned*)d_in[7], (const unsigned*)d_in[8], ws);
    k3_gemm1<<<256, 256, G1_LDS, stream>>>(ws + OFF_X1T, ws + OFF_W1T, (const float*)(ws + OFF_CS), ws + OFF_HT);
    k4_gemm2<false><<<256, 256, 0, stream>>>(ws + OFF_HT, ws + OFF_W2T, ws + OFF_X1T, ws + OFF_X2T);
    k5_mix<<<128, 64, 0, stream>>>((const u32x4*)(ws + OFF_X2T), (const u32x4*)(ws + OFF_YT), (u32x4*)(ws + OFF_X3T));
    k3_gemm1<<<256, 256, G1_LDS, stream>>>(ws + OFF_X3T, ws + OFF_W1T, (const float*)(ws + OFF_CS), ws + OFF_HT);
    k4_gemm2<true><<<256, 256, 0, stream>>>(ws + OFF_HT, ws + OFF_W2T, ws + OFF_X3T, (char*)d_out);
}
```

```cpp
#define WT1
#define WT2
#include <hip/hip_runtime.h>
#include <hip/hip_bf16.h>
#include <stdint.h>

typedef __attribute__((ext_vector_type(8))) short bf16x8;
typedef __attribute__((ext_vector_type(4))) float f32x4;
typedef __attribute__((ext_vector_type(2))) float f32x2;
typedef __attribute__((ext_vector_type(4))) unsigned int u32x4;
typedef __attribute__((ext_vector_type(2))) unsigned int u32x2;
typedef __attribute__((ext_vector_type(2))) __bf16 bf2_t;

constexpr int S_ = 2048, D_ = 640, F_ = 1728;
constexpr int KS1 = D_ / 32;
constexpr int KS2 = F_ / 32;
constexpr int NB1 = F_ / 16;
constexpr int DB_ = D_ / 16;

constexpr size_t SZ_ACT = (size_t)S_ * D_ * 2;
constexpr size_t OFF_URT = 0;
constexpr size_t OFF_X0T = OFF_URT + SZ_ACT;
constexpr size_t OFF_Q0T = OFF_X0T + SZ_ACT;
constexpr size_t OFF_X1T = OFF_Q0T + SZ_ACT;
constexpr size_t OFF_YT  = OFF_X1T + SZ_ACT;
constexpr size_t OFF_X2T = OFF_YT + SZ_ACT;
constexpr size_t OFF_X3T = OFF_X2T + SZ_ACT;
constexpr size_t OFF_HT  = OFF_X3T + SZ_ACT;
constexpr size_t SZ_HT   = (size_t)S_ * F_ * 2;
constexpr size_t OFF_W1T = OFF_HT + SZ_HT;
constexpr size_t SZ_W    = (size_t)D_ * F_ * 2;
constexpr size_t OFF_W2T = OFF_W1T + SZ_W;
constexpr size_t OFF_CS  = OFF_W2T + SZ_W;
constexpr size_t OFF_HT2 = OFF_CS + 8192;
constexpr size_t OFF_GR  = OFF_HT2 + SZ_HT;
constexpr size_t OFF_GR2 = OFF_GR + (size_t)S_ * 8 * 8;
constexpr size_t OFF_CNT = OFF_GR2 + (size_t)S_ * 8 * 8;
constexpr size_t WS_USED = OFF_CNT + 5 * 16 * 256;

#define STAMP_DECL
#define STAMP(ws_, kid, i) do {} while (0)
#define STAMP_FLUSH(ws_, kid) do {} while (0)
#define STAMP_END(ws_, kid, i) do {} while (0)
#define SB() __builtin_amdgcn_sched_barrier(0)

__device__ __forceinline__ float bflo(unsigned x) { return __uint_as_float(x << 16); }
__device__ __forceinline__ float bfhi(unsigned x) { return __uint_as_float(x & 0xffff0000u); }
__device__ __forceinline__ unsigned pk2(float lo, float hi) {
    f32x2 v = {lo, hi};
    bf2_t r = __builtin_convertvector(v, bf2_t);
    return __builtin_bit_cast(unsigned, r);
}
__device__ __forceinline__ float rbf(float x) { return bflo(pk2(x, 0.f)); }

__device__ __forceinline__ void gst16_wt(void* p, u32x4 v) {
    asm volatile("global_store_dwordx4 %0, %1, off sc1\n\ts_nop 1" :: "v"(p), "v"(v) : "memory");
}
__device__ __forceinline__ void gst8_wt(void* p, u32x2 v) {
    asm volatile("global_store_dwordx2 %0, %1, off sc1" :: "v"(p), "v"(v) : "memory");
}

__global__ __launch_bounds__(896) void k0_prep(const unsigned* __restrict__ q, const unsigned* __restrict__ k,
                                               const unsigned* __restrict__ v, const u32x4* __restrict__ shk,
                                               char* __restrict__ ws) {
    __shared__ __attribute__((aligned(16))) char xs[2][10240];
    const int b = blockIdx.x, t = threadIdx.x;
    const int s0 = b * 8;
    STAMP_DECL;
    STAMP(ws, 0, 0);
    if (t < 640) {
        const int cp = t % 320, rh = t / 320;
        const int sb = s0 + 4 * rh;
        unsigned kr[7], vr[4];
#pragma unroll
        for (int i = 0; i < 7; ++i) {
            int s = sb - 3 + i;
            kr[i] = k[(size_t)(s > 0 ? s : 0) * (D_ / 2) + cp];
        }
#pragma unroll
        for (int i = 0; i < 4; ++i) vr[i] = v[(size_t)(sb + i) * (D_ / 2) + cp];
        const u32x4 w = shk[cp];
        SB();
#pragma unroll
        for (int i = 0; i < 3; ++i) kr[i] = (sb - 3 + i >= 0) ? kr[i] : 0u;
        const float wa[4] = {bflo(w.x), bfhi(w.x), bflo(w.y), bfhi(w.y)};
        const float wb[4] = {bflo(w.z), bfhi(w.z), bflo(w.w), bfhi(w.w)};
        float ua[4], ub[4];
#pragma unroll
        for (int i = 0; i < 4; ++i) {
            unsigned acc = pk2(bflo(kr[i]) * wa[3], bfhi(kr[i]) * wb[3]);
#pragma unroll
            for (int j = 1; j < 4; ++j) {
                unsigned pr = pk2(bflo(kr[i + j]) * wa[3 - j], bfhi(kr[i + j]) * wb[3 - j]);
                acc = pk2(bflo(acc) + bflo(pr), bfhi(acc) + bfhi(pr));
            }
            ua[i] = bflo(acc) * bflo(vr[i]);
            ub[i] = bfhi(acc) * bfhi(vr[i]);
        }
        u32x2 oa = {pk2(ua[3], ua[2]), pk2(ua[1], ua[0])};
        u32x2 ob = {pk2(ub[3], ub[2]), pk2(ub[1], ub[0])};
        const int d = 2 * cp;
        char* dst = ws + OFF_URT + ((((size_t)(d >> 2) * 256 + (255 - b)) * 4 + (d & 3)) * 16) + (1 - rh) * 8;
        *(u32x2*)dst = oa;
        *(u32x2*)(dst + 16) = ob;
    } else {
        const int tt = t - 640;
        if (tt < 64) ((u32x2*)(ws + OFF_GR))[(size_t)s0 * 8 + tt] = (u32x2){0xffffffffu, 0xffffffffu};
        if (b == 0 && tt >= 64 && tt < 64 + 80) ((unsigned*)(ws + OFF_CNT))[(tt - 64) * 64] = 0u;
        const int row = tt >> 5, ll = tt & 31;
        const int s = s0 + row;
        u32x2 xq[5];
#pragma unroll
        for (int i = 0; i < 5; ++i) xq[i] = *(const u32x2*)(q + (size_t)s * (D_ / 2) + 2 * (ll + 32 * i));
        float xv[5][4];
        float sum = 0.f;
#pragma unroll
        for (int i = 0; i < 5; ++i) {
            xv[i][0] = bflo(xq[i].x); xv[i][1] = bfhi(xq[i].x); xv[i][2] = bflo(xq[i].y); xv[i][3] = bfhi(xq[i].y);
            sum += (xv[i][0] + xv[i][1]) + (xv[i][2] + xv[i][3]);
        }
#pragma unroll
        for (int o = 16; o; o >>= 1) sum += __shfl_xor(sum, o, 32);
        const float mean = sum * (1.f / D_);
        float vs = 0.f;
#pragma unroll
        for (int i = 0; i < 5; ++i)
#pragma unroll
            for (int e = 0; e < 4; ++e) { float a = xv[i][e] - mean; vs += a * a; }
#pragma unroll
        for (int o = 16; o; o >>= 1) vs += __shfl_xor(vs, o, 32);
        const float m = rbf(mean);
        const float dn = rbf(sqrtf(rbf(rbf(vs * (1.f / D_)) + 1e-5f)));
        const float rinv = 1.f / dn;
#pragma unroll
        for (int i = 0; i < 5; ++i) {
            const int g = ll + 32 * i;
            const int ks = g >> 3, half = (g >> 2) & 1, qq = g & 3;
            unsigned c01 = pk2(xv[i][0] - m, xv[i][1] - m), c23 = pk2(xv[i][2] - m, xv[i][3] - m);
            u32x2 o = {pk2(bflo(c01) * rinv, bfhi(c01) * rinv), pk2(bflo(c23) * rinv, bfhi(c23) * rinv)};
            const int lo = ((ks * 4 + qq) * 8 + row) * 16 + half * 8;
            *(u32x2*)(xs[0] + lo) = xq[i];
            *(u32x2*)(xs[1] + lo) = o;
        }
    }
    __syncthreads();
    if (t < 640) {
        const int kq = t >> 3, row = t & 7;
        const size_t off = (((size_t)(s0 >> 4) * KS1 + (kq >> 2)) * 64 + (kq & 3) * 16 + (s0 & 15) + row) * 16;
        *(u32x4*)(ws + OFF_X0T + off) = *(const u32x4*)(xs[0] + t * 16);
        *(u32x4*)(ws + OFF_Q0T + off) = *(const u32x4*)(xs[1] + t * 16);
    }
    STAMP(ws, 0, 1);
    STAMP_END(ws, 0, 2);
    STAMP_FLUSH(ws, 0);
}

constexpr int HC_STRIDE = 4160;
constexpr int CH_H = 4 * HC_STRIDE;
constexpr int UR_BYTES = 4608;
constexpr int CH_BYTES = CH_H + UR_BYTES;
constexpr int K1_RED_OFF = 4 * CH_BYTES;
constexpr int K1_LDS = K1_RED_OFF + 8 * 8192;

__device__ __forceinline__ void retile_block(const unsigned* __restrict__ W, int ncols, int nks, int cg, int u0, int u1,
                                             char* __restrict__ dstT, float* __restrict__ cs, float* lds) {
    const int t = threadIdx.x, lane = t & 63, wv = t >> 6;
    const int cp = lane & 31, hh = lane >> 5;
    const int n0 = cg * 64 + 2 * cp;
    float sa = 0.f, sb = 0.f;
    unsigned val[5][8];
#pragma unroll
    for (int i = 0; i < 5; ++i) {
        int u = u0 + wv * 10 + 2 * i + hh;
        int uu = u < u1 ? u : u1 - 1;
        int ks = uu >> 2, q = uu & 3;
#pragma unroll
        for (int j = 0; j < 8; ++j) {
            int kk = 32 * ks + (j >> 2) * 16 + 4 * q + (j & 3);
            val[i][j] = W[(size_t)kk * (ncols / 2) + (n0 >> 1)];
        }
    }
#pragma unroll
    for (int i = 0; i < 5; ++i) {
        int u = u0 + wv * 10 + 2 * i + hh;
        if (u < u1) {
            int ks = u >> 2, q = u & 3;
            u32x4 lo, hi;
            lo.x = __builtin_amdgcn_perm(val[i][1], val[i][0], 0x05040100u);
            lo.y = __builtin_amdgcn_perm(val[i][3], val[i][2], 0x05040100u);
            lo.z = __builtin_amdgcn_perm(val[i][5], val[i][4], 0x05040100u);
            lo.w = __builtin_amdgcn_perm(val[i][7], val[i][6], 0x05040100u);
            hi.x = __builtin_amdgcn_perm(val[i][1], val[i][0], 0x07060302u);
            hi.y = __builtin_amdgcn_perm(val[i][3], val[i][2], 0x07060302u);
            hi.z = __builtin_amdgcn_perm(val[i][5], val[i][4], 0x07060302u);
            hi.w = __builtin_amdgcn_perm(val[i][7], val[i][6], 0x07060302u);
            int nb = n0 >> 4, r = n0 & 15;
            u32x4* dst = (u32x4*)(dstT + (((size_t)nb * nks + ks) * 64 + q * 16 + r) * 16);
            gst16_wt(dst, lo);
            gst16_wt(dst + 1, hi);
#pragma unroll
            for (int j = 0; j < 8; ++j) { sa += bflo(val[i][j]); sb += bfhi(val[i][j]); }
        }
    }
    if (cs) {
        lds[(wv * 2 + hh) * 64 + 2 * cp] = sa;
        lds[(wv * 2 + hh) * 64 + 2 * cp + 1] = sb;
        __syncthreads();
        if (t < 64) {
            float s = 0.f;
#pragma unroll
            for (int i = 0; i < 16; ++i) s += lds[i * 64 + t];
            cs[cg * 64 + t] = s;
        }
    }
}

__global__ __launch_bounds__(512) void k1_conv(const unsigned short* __restrict__ ssmk, const unsigned short* __restrict__ ssmD,
                                               const unsigned* __restrict__ w1, const unsigned* __restrict__ w2,
                                               char* __restrict__ ws) {
    extern __shared__ __attribute__((aligned(16))) char smem[];
    const int b = blockIdx.x, t = threadIdx.x;
    if (b >= 160) {
        STAMP_DECL;
        STAMP(ws, 7, 0);
        int r = b - 160;
        if (r < 27) {
            retile_block(w1, F_, KS1, r, 0, 80, ws + OFF_W1T, (float*)(ws + OFF_CS), (float*)smem);
        } else {
            r -= 27;
            int cg = r / 3, part = r % 3;
            int u0 = part * 80, u1 = (u0 + 80 < 216) ? u0 + 80 : 216;
            retile_block(w2, D_, KS2, cg, u0, u1, ws + OFF_W2T, nullptr, (float*)smem);
        }
        STAMP_END(ws, 7, 1);
        STAMP_FLUSH(ws, 7);
        return;
    }
    STAMP_DECL;
    STAMP(ws, 1, 0);
    const int d0 = 4 * b;
    const int lane = t & 63, wv = __builtin_amdgcn_readfirstlane(t >> 6);
    {
        const u32x4* src = (const u32x4*)(ws + OFF_URT) + (size_t)b * 1024;
        const int hch = t >> 7, hi = t & 127;
        const unsigned short* hp = ssmk + (size_t)(d0 + hch) * S_;
        u32x4 ha[2]; u32x2 hb[2];
#pragma unroll
        for (int e = 0; e < 2; ++e) {
            const int m = hi + 128 * e;
            ha[e] = *(const u32x4*)(hp + 8 * m);
            hb[e] = *(const u32x2*)(hp + (m < 255 ? 8 * m + 8 : 0));
        }
        u32x4 a0 = src[t], a1 = src[t + 512];
        SB();
        char* cbase = smem + hch * CH_BYTES;
        if (hi == 0) {
            u32x4 z = {0u, 0u, 0u, 0u};
            u32x4 c1 = z, c2 = z, c3 = z;
            c1.w = ha[0].x << 16;
            c2.w = ha[0].x;
            c3.z = ha[0].x << 16; c3.w = __builtin_amdgcn_alignbit(ha[0].y, ha[0].x, 16);
            *(u32x4*)(cbase + 16) = z;
            *(u32x4*)(cbase + 16 + HC_STRIDE) = c1;
            *(u32x4*)(cbase + 16 + 2 * HC_STRIDE) = c2;
            *(u32x4*)(cbase + 16 + 3 * HC_STRIDE) = c3;
        } else if (hi < 5) {
            *(u32x4*)(cbase + (hi - 1) * HC_STRIDE) = (u32x4){0u, 0u, 0u, 0u};
        } else if (hi < 37) {
            *(u32x4*)(cbase + CH_H + 4096 + (hi - 5) * 16) = (u32x4){0u, 0u, 0u, 0u};
        }
#pragma unroll
        for (int e = 0; e < 2; ++e) {
            char* base = cbase + (hi + 128 * e + 2) * 16;
            u32x4 A = ha[e]; u32x2 B = hb[e];
            u32x4 c1 = {__builtin_amdgcn_alignbit(A.y, A.x, 16), __builtin_amdgcn_alignbit(A.z, A.y, 16),
                        __builtin_amdgcn_alignbit(A.w, A.z, 16), __builtin_amdgcn_alignbit(B.x, A.w, 16)};
            u32x4 c2 = {A.y, A.z, A.w, B.x};
            u32x4 c3 = {c1.y, c1.z, c1.w, __builtin_amdgcn_alignbit(B.y, B.x, 16)};
            *(u32x4*)(base) = A;
            *(u32x4*)(base + HC_STRIDE) = c1;
            *(u32x4*)(base + 2 * HC_STRIDE) = c2;
            *(u32x4*)(base + 3 * HC_STRIDE) = c3;
        }
        *(u32x4*)(smem + (t & 3) * CH_BYTES + CH_H + (t >> 2) * 16) = a0;
        *(u32x4*)(smem + (t & 3) * CH_BYTES + CH_H + ((t + 512) >> 2) * 16) = a1;
    }
    const int eks = d0 >> 5, ehalf = (d0 >> 4) & 1, eq = (d0 >> 2) & 3;
    u32x2 x0r[4], q0r[4];
#pragma unroll
    for (int i = 0; i < 4; ++i) {
        int s = t + 512 * i;
        size_t off = (((size_t)(s >> 4) * KS1 + eks) * 64 + eq * 16 + (s & 15)) * 16 + ehalf * 8;
        x0r[i] = *(const u32x2*)(ws + OFF_X0T + off);
        q0r[i] = *(const u32x2*)(ws + OFF_Q0T + off);
    }
    const u32x2 dsk = *(const u32x2*)(ssmD + d0);
    SB();
    __syncthreads();
    STAMP(ws, 1, 1);
    {
        const int ch = wv & 3, bh = wv >> 2;
        const int jr = lane & 15, q = lane >> 4;
        const unsigned sbase = (unsigned)(size_t)(__attribute__((address_space(3))) char*)smem;
        const unsigned abase = sbase + ch * CH_BYTES + ((jr + 1) & 3) * HC_STRIDE + 16 * q + 2 * ((jr + 1) & ~3) + 64 * (4 * bh);
        const unsigned bbase = sbase + ch * CH_BYTES + CH_H + 2 * (2032 - 16 * jr + 8 * q) + 64 * (4 * bh) - 512 * 7;
        f32x4 acc[8];
#pragma unroll
        for (int c = 0; c < 8; ++c) acc[c] = (f32x4){0.f, 0.f, 0.f, 0.f};
        u32x2 Al[2][8], Ah[2][8];
        bf16x8 Bf[2][8];
#define K1_RA(st, a_, bb_)                                                                                      \
        asm volatile("ds_read_b64 %0, %1 offset:%2" : "=v"(Al[st][a_]) : "v"(abase), "i"(64 * (bb_) + 512 * (a_)) : "memory"); \
        asm volatile("ds_read_b64 %0, %1 offset:%2" : "=v"(Ah[st][a_]) : "v"(abase), "i"(64 * (bb_) + 512 * (a_) + 8) : "memory");
#define K1_RB(st, m_, bb_)                                                                                      \
        asm volatile("ds_read_b128 %0, %1 offset:%2" : "=v"(Bf[st][m_]) : "v"(bbase), "i"(64 * (bb_) + 512 * (7 - (m_))) : "memory");
#define K1_WAIT(st)                                                                                             \
        asm volatile("s_waitcnt lgkmcnt(0)"                                                                     \
                     : "+v"(Al[st][0]), "+v"(Al[st][1]), "+v"(Al[st][2]), "+v"(Al[st][3]), "+v"(Al[st][4]), "+v"(Al[st][5]), "+v"(Al[st][6]), "+v"(Al[st][7]), \
                       "+v"(Ah[st][0]), "+v"(Ah[st][1]), "+v"(Ah[st][2]), "+v"(Ah[st][3]), "+v"(Ah[st][4]), "+v"(Ah[st][5]), "+v"(Ah[st][6]), "+v"(Ah[st][7]) \
                     :: "memory");                                                                              \
        asm volatile(""                                                                                         \
                     : "+v"(Bf[st][0]), "+v"(Bf[st][1]), "+v"(Bf[st][2]), "+v"(Bf[st][3]), "+v"(Bf[st][4]), "+v"(Bf[st][5]), "+v"(Bf[st][6]), "+v"(Bf[st][7]) \
                     :: "memory");
#define K1_MROW(st, a_)                                                                                         \
        {                                                                                                       \
            u32x4 x_ = {Al[st][a_].x, Al[st][a_].y, Ah[st][a_].x, Ah[st][a_].y};                                \
            bf16x8 af_ = __builtin_bit_cast(bf16x8, x_);                                                        \
            _Pragma("unroll") for (int m = 0; m < 8 - (a_); ++m)                                                \
                acc[(a_) + m] = __builtin_amdgcn_mfma_f32_16x16x32_bf16(af_, Bf[st][m], acc[(a_) + m], 0, 0, 0); \
            SB();                                                                                               \
        }
#define K1_ITER(st, bb_, NEXT)                                                                                  \
        K1_WAIT(st)                                                                                             \
        K1_MROW(st, 0) if (NEXT) { K1_RB((st) ^ 1, 0, (bb_) + 1) K1_RB((st) ^ 1, 1, (bb_) + 1) K1_RB((st) ^ 1, 2, (bb_) + 1) K1_RB((st) ^ 1, 3, (bb_) + 1) K1_RA((st) ^ 1, 0, (bb_) + 1) } SB(); \
        K1_MROW(st, 1) if (NEXT) { K1_RB((st) ^ 1, 4, (bb_) + 1) K1_RB((st) ^ 1, 5, (bb_) + 1) K1_RB((st) ^ 1, 6, (bb_) + 1) K1_RB((st) ^ 1, 7, (bb_) + 1) K1_RA((st) ^ 1, 1, (bb_) + 1) } SB(); \
        K1_MROW(st, 2) if (NEXT) { K1_RA((st) ^ 1, 2, (bb_) + 1) K1_RA((st) ^ 1, 3, (bb_) + 1) } SB();          \
        K1_MROW(st, 3) if (NEXT) { K1_RA((st) ^ 1, 4, (bb_) + 1) K1_RA((st) ^ 1, 5, (bb_) + 1) } SB();          \
        K1_MROW(st, 4) if (NEXT) { K1_RA((st) ^ 1, 6, (bb_) + 1) K1_RA((st) ^ 1, 7, (bb_) + 1) } SB();          \
        K1_MROW(st, 5) K1_MROW(st, 6) K1_MROW(st, 7)
        K1_RB(0, 0, 0) K1_RA(0, 0, 0) K1_RB(0, 1, 0) K1_RB(0, 2, 0) K1_RB(0, 3, 0) K1_RB(0, 4, 0) K1_RB(0, 5, 0) K1_RB(0, 6, 0) K1_RB(0, 7, 0)
        K1_RA(0, 1, 0) K1_RA(0, 2, 0) K1_RA(0, 3, 0) K1_RA(0, 4, 0) K1_RA(0, 5, 0) K1_RA(0, 6, 0) K1_RA(0, 7, 0)
        K1_ITER(0, 0, true)
        K1_ITER(1, 1, true)
        K1_ITER(0, 2, true)
        K1_ITER(1, 3, false)
        float* red = (float*)(smem + K1_RED_OFF) + (ch * 2 + bh) * 2048;
#pragma unroll
        for (int c = 0; c < 8; ++c) *(f32x4*)(red + 256 * c + 16 * jr + 4 * (q ^ ((jr >> 1) & 3))) = acc[c];
    }
    __syncthreads();
    STAMP(ws, 1, 2);
    {
        const float* red = (const float*)(smem + K1_RED_OFF);
        float dk[4] = {bflo(dsk.x), bfhi(dsk.x), bflo(dsk.y), bfhi(dsk.y)};
#pragma unroll
        for (int i = 0; i < 4; ++i) {
            int s = t + 512 * i;
            const int sr = s ^ (((s >> 5) & 3) << 2);
            float xs[4] = {bflo(x0r[i].x), bfhi(x0r[i].x), bflo(x0r[i].y), bfhi(x0r[i].y)};
            float qs[4] = {bflo(q0r[i].x), bfhi(q0r[i].x), bflo(q0r[i].y), bfhi(q0r[i].y)};
            float yo[4], xo[4];
#pragma unroll
            for (int c = 0; c < 4; ++c) {
                float y = rbf(red[(c * 2) * 2048 + sr] + red[(c * 2 + 1) * 2048 + sr]);
                unsigned short uu = *(const unsigned short*)(smem + c * CH_BYTES + CH_H + 2 * (2047 - s));
                float u = bflo(uu);
                y = rbf(y + rbf(u * dk[c]));
                yo[c] = y;
                xo[c] = xs[c] + rbf(qs[c] * y);
            }
            size_t off = (((size_t)(s >> 4) * KS1 + eks) * 64 + eq * 16 + (s & 15)) * 16 + ehalf * 8;
            u32x2 oy = {pk2(yo[0], yo[1]), pk2(yo[2], yo[3])};
            u32x2 ox = {pk2(xo[0], xo[1]), pk2(xo[2], xo[3])};
            gst8_wt(ws + OFF_YT + off, oy);
            gst8_wt(ws + OFF_X1T + off, ox);
        }
    }
    STAMP(ws, 1, 3);
    STAMP_END(ws, 1, 4);
    STAMP_FLUSH(ws, 1);
}

constexpr int G1_LDS = KS1 * 7 * 1024;
constexpr int FFN_LDS = G1_LDS + 20480;
constexpr unsigned SPIN_CAP = 1u << 22;
typedef __attribute__((address_space(1))) unsigned gu32;
typedef __attribute__((address_space(1))) unsigned long long gu64;

#define GLOAD_X4(dst, voff, sbase, imm) \
    asm volatile("s_nop 4\n\tglobal_load_dwordx4 %0, %1, %2 offset:%3 sc1" : "=v"(dst) : "v"(voff), "s"(sbase), "i"(imm) : "memory")
#define DSR(dst, addr, off) asm volatile("ds_read_b128 %0, %1 offset:%2" : "=v"(dst) : "v"(addr), "i"(off) : "memory")
__device__ __forceinline__ void store16_sc1(u32x4 v, const __amdgpu_buffer_rsrc_t rs, unsigned voff, int soff) {
    __builtin_amdgcn_raw_buffer_store_b128(v, rs, voff, soff, 16);
    asm volatile("s_nop 1" :: "v"(v) : "memory");
}
#define VMWAIT(n) asm volatile("s_waitcnt vmcnt(" #n ")" ::: "memory")

__device__ __forceinline__ void wait_count(char* ws, int idx, unsigned want) {
    if (threadIdx.x == 0) {
        gu32* c = (gu32*)(ws + OFF_CNT + (size_t)idx * 256);
        unsigned spins = 0;
        while (__hip_atomic_load(c, __ATOMIC_RELAXED, __HIP_MEMORY_SCOPE_AGENT) < want) {
            if (++spins > SPIN_CAP) break;
        }
        __builtin_amdgcn_fence(__ATOMIC_ACQUIRE, "wavefront");
    }
    __builtin_amdgcn_s_barrier();
    asm volatile("" ::: "memory");
}
__device__ __forceinline__ void publish_count(char* ws, int idx) {
    __builtin_amdgcn_s_barrier();
    if (threadIdx.x == 0)
        __hip_atomic_fetch_add((gu32*)(ws + OFF_CNT + (size_t)idx * 256), 1u, __ATOMIC_RELAXED, __HIP_MEMORY_SCOPE_AGENT);
}

constexpr int G2_PH = 4;
constexpr int G2_NAG = 52;
constexpr int G2_KEEP = 9;
__device__ __forceinline__ int nb_of(int ct, int i) { int j = i + (ct & 1); j = j >= 7 ? j - 7 : j; return 7 * ct + j; }
__device__ __forceinline__ int db_of(int cu, int j) { int t = j + (cu & 1); t = t >= 5 ? t - 5 : t; return 5 * cu + t; }

__device__ __forceinline__ int ksA_of(int a) { return 7 * (a >> 2) + (a & 1) + ((a & 2) << 1); }
__device__ __forceinline__ int ksB_of(int b) { int g = b / 3, r = b - 3 * g; return 7 * g + (r == 2 ? 6 : r + 2); }

template <int L>
__device__ __forceinline__ void ffn_layer(char* __restrict__ ws, char* __restrict__ out, char* smem, const __amdgpu_buffer_rsrc_t rs,
                                          const int rp, const int ct, const int rq, const int cu, const int lane, const int wv,
                                          u32x2 (&xres)[5], bf16x8 (&A)[14][5]
                                          ) {
    const int rb0 = rp * 8 + 2 * wv;
    const int q = lane >> 4;
    const unsigned voff = lane * 16;
    int kst[14];
#pragma unroll
    for (int i = 0; i < 8; ++i) { int a = 8 * wv + i; kst[i] = ksA_of(a < 31 ? a : 30); }
#pragma unroll
    for (int i = 0; i < 6; ++i) { int bb = (wv < 3 ? 6 * wv : 17) + i; if (wv == 2 && i == 5) bb = 16; kst[8 + i] = ksB_of(bb); }
    int wsoff[5];
#pragma unroll
    for (int j = 0; j < 5; ++j) wsoff[j] = db_of(cu, j) * KS2 * 1024;
    const char* w2base = ws + OFF_W2T;
    auto loadA1 = [&](int f) {
        const int g = f / 5, j = f % 5;
        if (f < 70 && (L == 0 || g >= G2_KEEP)) {
            const char* pb = w2base + (wsoff[j] + kst[g] * 1024);
            if (f < G2_NAG) asm volatile("s_nop 4\n\tglobal_load_dwordx4 %0, %1, %2" : "=a"(A[g][j]) : "v"(voff), "s"(pb) : "memory");
            else        asm volatile("s_nop 4\n\tglobal_load_dwordx4 %0, %1, %2" : "=v"(A[g][j]) : "v"(voff), "s"(pb) : "memory");
        }
    };
    constexpr int F0 = (L == 0) ? 0 : 5 * G2_KEEP;
    const int hoff = (int)(L == 0 ? OFF_HT : OFF_HT2);
    {
        const char* xbase = ws + (L == 0 ? OFF_X1T : OFF_X3T) + (size_t)rb0 * (KS1 * 1024);
        const char* W1T = ws + OFF_W1T;
        f32x4 csv[7];
#pragma unroll
        for (int i = 0; i < 7; ++i) {
            int nb = nb_of(ct, i); nb = nb < NB1 ? nb : NB1 - 1;
            csv[i] = __builtin_bit_cast(f32x4, __builtin_amdgcn_raw_buffer_load_b128(rs, q * 16, (int)OFF_CS + nb * 64, 0));
        }
        SB();
        bf16x8 xf[2][KS1];
        u32x4 pst[2];
        const unsigned voffB = voff + KS1 * 1024;
#define LX(r, ks) { unsigned v_ = ((r) ? voffB : voff) + ((ks) >> 2) * 4096; GLOAD_X4(xf[r][ks], v_, xbase, ((ks) & 3) * 1024); }
#define LW(c, i)                                                                                                \
    {                                                                                                           \
        int nb = nb_of(ct, i); nb = nb < NB1 ? nb : NB1 - 1;                                                    \
        int ks_ = 4 * (c) + wv;                                                                                 \
        __builtin_amdgcn_global_load_lds((const unsigned*)(W1T + ((size_t)nb * KS1 + ks_) * 1024 + lane * 16),  \
                                         (unsigned*)(smem + (ks_ * 7 + (i)) * 1024), 16, 0, 0);                 \
    }
#define ISSUE_X(c) LX(0, 4 * (c)) LX(1, 4 * (c)) LX(0, 4 * (c) + 1) LX(1, 4 * (c) + 1) LX(0, 4 * (c) + 2) LX(1, 4 * (c) + 2) LX(0, 4 * (c) + 3) LX(1, 4 * (c) + 3)
#define ISSUE_WA(c) LW(c, 0) LW(c, 1) LW(c, 2) LW(c, 3)
#define WAIT_X(c, n)                                                                                           \
    asm volatile("s_waitcnt vmcnt(" #n ")"                                                                      \
                 : "+v"(xf[0][4 * (c)]), "+v"(xf[0][4 * (c) + 1]), "+v"(xf[0][4 * (c) + 2]), "+v"(xf[0][4 * (c) + 3]), \
                   "+v"(xf[1][4 * (c)]), "+v"(xf[1][4 * (c) + 1]), "+v"(xf[1][4 * (c) + 2]), "+v"(xf[1][4 * (c) + 3])  \
                 :: "memory");
#define BARR() __builtin_amdgcn_s_barrier(); asm volatile("" ::: "memory");
#define WAITL4(w) asm volatile("s_waitcnt lgkmcnt(0)" : "+v"(w[0]), "+v"(w[1]), "+v"(w[2]), "+v"(w[3]) :: "memory")
#define WAITL3(w) asm volatile("s_waitcnt lgkmcnt(0)" : "+v"(w[0]), "+v"(w[1]), "+v"(w[2]) :: "memory")
#define MM2(wf, wi, ks, i)                                                                                      \
    acc[0][i] = __builtin_amdgcn_mfma_f32_16x16x32_bf16(wf[wi], xf[0][ks], acc[0][i], 0, 0, 0);                 \
    acc[1][i] = __builtin_amdgcn_mfma_f32_16x16x32_bf16(wf[wi], xf[1][ks], acc[1][i], 0, 0, 0);
#define LDW1(dst, di, ks, i) { DSR(dst[di], lbase + ((ks) * 7168 / 57344) * 57344, ((ks) * 7168) % 57344 + (i) * 1024); }
#define STEPA(cur, nxt, ks, PRE, S0, S1, S2, S3)                                                                \
    {                                                                                                           \
        PRE                                                                                                     \
        WAITL4(cur);                                                                                            \
        MM2(cur, 0, ks, 0) SB(); if ((ks) < 19) LDW1(nxt, 0, (ks) + 1, 0) else LDW1(nxt, 0, 0, 4) S0 SB();      \
        MM2(cur, 1, ks, 1) SB(); if ((ks) < 19) LDW1(nxt, 1, (ks) + 1, 1) else LDW1(nxt, 1, 0, 5) S1 SB();      \
        MM2(cur, 2, ks, 2) SB(); if ((ks) < 19) LDW1(nxt, 2, (ks) + 1, 2) else LDW1(nxt, 2, 0, 6) S2 SB();      \
        if ((ks) < 19) LDW1(nxt, 3, (ks) + 1, 3) SB();                                                          \
        MM2(cur, 3, ks, 3)                                                                                      \
        if (L == 0) {                                                                                           \
            asum[0] = __builtin_amdgcn_mfma_f32_16x16x32_bf16(ones, xf[0][ks], asum[0], 0, 0, 0);               \
            asum[1] = __builtin_amdgcn_mfma_f32_16x16x32_bf16(ones, xf[1][ks], asum[1], 0, 0, 0);               \
            SB(); S3 SB();                                                                                      \
            asq[0] = __builtin_amdgcn_mfma_f32_16x16x32_bf16(xf[0][ks], xf[0][ks], asq[0], 0, 0, 0);            \
            asq[1] = __builtin_amdgcn_mfma_f32_16x16x32_bf16(xf[1][ks], xf[1][ks], asq[1], 0, 0, 0);            \
            SB();                                                                                               \
        } else { SB(); S3 SB(); }                                                                               \
    }
#define CHUNKA_XW(c, cn, PRE3)                                                                                  \
        STEPA(wA, wB, 4 * (c), , LX(0, 4 * (cn)), LX(1, 4 * (cn)), LX(0, 4 * (cn) + 1), LX(1, 4 * (cn) + 1))    \
        STEPA(wB, wA, 4 * (c) + 1, , LX(0, 4 * (cn) + 2), LX(1, 4 * (cn) + 2), LX(0, 4 * (cn) + 3), LX(1, 4 * (cn) + 3)) \
        STEPA(wA, wB, 4 * (c) + 2, , LW(cn, 0), LW(cn, 1), LW(cn, 2), LW(cn, 3))                                \
        STEPA(wB, wA, 4 * (c) + 3, PRE3, , , , )
#define CHUNKA_X(c, cn, PRE3)                                                                                   \
        STEPA(wA, wB, 4 * (c), , LX(0, 4 * (cn)), LX(1, 4 * (cn)), LX(0, 4 * (cn) + 1), )                       \
        STEPA(wB, wA, 4 * (c) + 1, , LX(1, 4 * (cn) + 1), LX(0, 4 * (cn) + 2), LX(1, 4 * (cn) + 2), )           \
        STEPA(wA, wB, 4 * (c) + 2, , LX(0, 4 * (cn) + 3), LX(1, 4 * (cn) + 3), , )                              \
        STEPA(wB, wA, 4 * (c) + 3, PRE3, , , , )
#define CHUNKA_0(c, PRE3)                                                                                       \
        STEPA(wA, wB, 4 * (c), , , , , )                                                                        \
        STEPA(wB, wA, 4 * (c) + 1, , , , , )                                                                    \
        STEPA(wA, wB, 4 * (c) + 2, , , , , )                                                                    \
        STEPA(wB, wA, 4 * (c) + 3, PRE3, , , , )
        const unsigned lbase = (unsigned)(size_t)(__attribute__((address_space(3))) char*)smem + lane * 16;
        f32x4 acc[2][7];
#pragma unroll
        for (int i = 0; i < 7; ++i) { acc[0][i] = (f32x4){0.f, 0.f, 0.f, 0.f}; acc[1][i] = (f32x4){0.f, 0.f, 0.f, 0.f}; }
        f32x4 asum[2] = {(f32x4){0.f, 0.f, 0.f, 0.f}, (f32x4){0.f, 0.f, 0.f, 0.f}};
        f32x4 asq[2] = {(f32x4){0.f, 0.f, 0.f, 0.f}, (f32x4){0.f, 0.f, 0.f, 0.f}};
        const bf16x8 ones = {0x3f80, 0x3f80, 0x3f80, 0x3f80, 0x3f80, 0x3f80, 0x3f80, 0x3f80};
        bf16x8 wA[4], wB[4];
        if (L == 0) {
            ISSUE_X(0) ISSUE_WA(0) ISSUE_X(1) ISSUE_WA(1)
            WAIT_X(0, 12) BARR()
            STAMP(ws, 2, 1);
            LDW1(wA, 0, 0, 0) LDW1(wA, 1, 0, 1) LDW1(wA, 2, 0, 2) LDW1(wA, 3, 0, 3)
            CHUNKA_XW(0, 2, WAIT_X(1, 12) BARR())
            CHUNKA_XW(1, 3, WAIT_X(2, 12) BARR())
            CHUNKA_XW(2, 4, WAIT_X(3, 12) BARR())
            STEPA(wA, wB, 12, , LW(0, 4), LW(0, 5), LW(0, 6), LW(1, 4))
            STEPA(wB, wA, 13, , LW(1, 5), LW(1, 6), LW(2, 4), LW(2, 5))
            STEPA(wA, wB, 14, , LW(2, 6), LW(3, 4), LW(3, 5), LW(3, 6))
            STEPA(wB, wA, 15, WAIT_X(4, 12) BARR(), LW(4, 4), LW(4, 5), LW(4, 6), )
            STEPA(wA, wB, 16, , , , , )
            STEPA(wB, wA, 17, , , , , )
            STEPA(wA, wB, 18, , , , , )
            STEPA(wB, wA, 19, VMWAIT(0); BARR(), , , , )
        } else {
            ISSUE_X(0) ISSUE_X(1)
            {
                const char* gb = ws + OFF_GR2 + (size_t)rb0 * 1024;
                unsigned vo = (lane & 15) * 64 + q * 16;
                GLOAD_X4(pst[0], vo, gb, 0);
                GLOAD_X4(pst[1], vo, gb, 1024);
            }
            LDW1(wA, 0, 0, 0) LDW1(wA, 1, 0, 1) LDW1(wA, 2, 0, 2) LDW1(wA, 3, 0, 3)
            WAIT_X(0, 10)
            STAMP(ws, 2, 9);
            CHUNKA_X(0, 2, WAIT_X(1, 10))
            CHUNKA_X(1, 3, WAIT_X(2, 8))
            CHUNKA_X(2, 4, WAIT_X(3, 8))
            CHUNKA_0(3, WAIT_X(4, 0))
            asm volatile("" : "+v"(pst[0]), "+v"(pst[1]));
            CHUNKA_0(4, )
        }
        STAMP(ws, 2, L * 8 + 2);
        float mm[2], ri[2];
#pragma unroll
        for (int r = 0; r < 2; ++r) {
            float mean, var;
            if (L == 0) {
                const int tk = lane & 15;
                float dsel = (tk & 2) ? ((tk & 1) ? asq[r][3] : asq[r][2]) : ((tk & 1) ? asq[r][1] : asq[r][0]);
                float bq = __shfl(dsel, tk + 16 * (tk >> 2));
                mean = asum[r][0] * (1.f / D_);
                var = fmaxf(bq * (1.f / D_) - mean * mean, 0.f);
            } else {
                const float s0 = __uint_as_float(pst[r].x), s1 = __uint_as_float(pst[r].z);
                float st = s0 + s1;
                st += __shfl_xor(st, 16); st += __shfl_xor(st, 32);
                mean = st * (1.f / D_);
                const float d0 = s0 * (1.f / 80.f) - mean, d1 = s1 * (1.f / 80.f) - mean;
                float vt = (__uint_as_float(pst[r].y) + __uint_as_float(pst[r].w)) + 80.f * (d0 * d0 + d1 * d1);
                vt += __shfl_xor(vt, 16); vt += __shfl_xor(vt, 32);
                var = vt * (1.f / D_);
            }
            mm[r] = rbf(mean);
            ri[r] = 1.f / rbf(sqrtf(rbf(rbf(var) + 1e-5f)));
        }
        asm volatile("s_waitcnt lgkmcnt(0)" : "+v"(mm[0]), "+v"(mm[1]), "+v"(ri[0]), "+v"(ri[1]) :: "memory");
        SB();
        u32x2 hst[2][7];
        auto epi = [&](int r, int i) {
            const float m = mm[r], rinv = ri[r], rz = rinv * -1.44269504089f;
            f32x2 a0 = {acc[r][i][0], acc[r][i][1]}, a1 = {acc[r][i][2], acc[r][i][3]};
            f32x2 c0 = {csv[i][0], csv[i][1]}, c1 = {csv[i][2], csv[i][3]};
            f32x2 t0 = a0 - m * c0, t1 = a1 - m * c1;
            f32x2 h0 = t0 * rinv, h1 = t1 * rinv;
            f32x2 z0 = t0 * rz, z1 = t1 * rz;
            f32x2 e0 = {__builtin_amdgcn_exp2f(z0.x), __builtin_amdgcn_exp2f(z0.y)};
            f32x2 e1 = {__builtin_amdgcn_exp2f(z1.x), __builtin_amdgcn_exp2f(z1.y)};
            e0 += 1.f; e1 += 1.f;
            f32x2 r0 = {__builtin_amdgcn_rcpf(e0.x), __builtin_amdgcn_rcpf(e0.y)};
            f32x2 r1 = {__builtin_amdgcn_rcpf(e1.x), __builtin_amdgcn_rcpf(e1.y)};
            h0 *= r0; h1 *= r1;
            hst[r][i].x = pk2(h0.x, h0.y); hst[r][i].y = pk2(h1.x, h1.y);
        };
        auto store_pair = [&](int r, int i) {
            const int nb = nb_of(ct, i);
            u32x4 s4 = {hst[r][i].x, hst[r][i].y, hst[r][i + 1].x, hst[r][i + 1].y};
            const int so = nb < NB1 ? hoff + ((rb0 + r) * KS2 + (nb >> 1)) * 1024 : 0x7ffffc00;
            store16_sc1(s4, rs, voff, so);
        };
#define STEPB(cur, nxt, ks, EPI, POST)                                                                          \
    {                                                                                                           \
        WAITL3(cur);                                                                                            \
        if ((ks) < 19) { LDW1(nxt, 0, (ks) + 1, 4) LDW1(nxt, 1, (ks) + 1, 5) LDW1(nxt, 2, (ks) + 1, 6) }        \
        SB();                                                                                                   \
        MM2(cur, 0, ks, 4) MM2(cur, 1, ks, 5) MM2(cur, 2, ks, 6)                                                \
        EPI                                                                                                     \
        _Pragma("unroll") for (int z_ = 0; z_ < 6; ++z_) {                                                      \
            __builtin_amdgcn_sched_group_barrier(0x8, 1, 0);                                                    \
            __builtin_amdgcn_sched_group_barrier(0x2, 4, 0);                                                    \
        }                                                                                                       \
        SB();                                                                                                   \
        POST                                                                                                    \
        loadA1(3 * (ks)); loadA1(3 * (ks) + 1); loadA1(3 * (ks) + 2);                                           \
        SB();                                                                                                   \
    }
#define NA_IN(lo, hi) ((3 * (hi) < F0 ? F0 : 3 * (hi)) - (3 * (lo) < F0 ? F0 : 3 * (lo)))
        STEPB(wA, wB, 0, epi(0, 0);, )
        STEPB(wB, wA, 1, epi(0, 1);, store_pair(0, 0);)
        STEPB(wA, wB, 2, epi(0, 2);, )
        STEPB(wB, wA, 3, epi(0, 3);, store_pair(0, 2);)
        STEPB(wA, wB, 4, epi(1, 0);, )
        STEPB(wB, wA, 5, epi(1, 1);, store_pair(1, 0);)
        STEPB(wA, wB, 6, epi(1, 2);, )
        STEPB(wB, wA, 7, epi(1, 3);, store_pair(1, 2);)
        STEPB(wA, wB, 8, , )
        STEPB(wB, wA, 9, , )
        STEPB(wA, wB, 10, , )
        STEPB(wB, wA, 11, , )
        STEPB(wA, wB, 12, , asm volatile("s_waitcnt vmcnt(%0)" :: "n"(NA_IN(7, 12)) : "memory"); publish_count(ws, L * 32 + rp);)
        STEPB(wB, wA, 13, , )
        STEPB(wA, wB, 14, , )
        STEPB(wB, wA, 15, , )
        STEPB(wA, wB, 16, , )
        STEPB(wB, wA, 17, , )
        STEPB(wA, wB, 18, , )
        STEPB(wB, wA, 19, , )
        STAMP(ws, 2, L * 8 + 3);
#pragma unroll
        for (int r = 0; r < 2; ++r) {
            epi(r, 4); epi(r, 5);
            store_pair(r, 4);
            epi(r, 6);
            const int nb = nb_of(ct, 6);
            __builtin_amdgcn_raw_buffer_store_b64(hst[r][6], rs, voff + (nb & 1) * 8, hoff + ((rb0 + r) * KS2 + (nb >> 1)) * 1024, 16);
        }
        SB();
#pragma unroll
        for (int f = 60; f < 70; ++f) loadA1(f);
        SB();
        asm volatile("s_waitcnt vmcnt(%0)" :: "n"(F0 > 60 ? 70 - F0 : 10) : "memory");
    }
    publish_count(ws, L * 32 + 16 + rp);
    STAMP(ws, 2, L * 8 + 4);
    wait_count(ws, L * 32 + (rq >> 1), 16u);
    STAMP(ws, 2, L * 8 + 5);
    {
        int hoffc[4];
#pragma unroll
        for (int c = 0; c < 4; ++c) hoffc[c] = hoff + (rq * 4 + ((wv + c) & 3)) * (KS2 * 1024);
        u32x2 yv[5];
        if (L == 0) {
#pragma unroll
            for (int j = 0; j < 5; ++j) {
                const int db = db_of(cu, j);
                const int so = ((rq * 4 + wv) * KS1 + (db >> 1)) * 1024 + (db & 1) * 8;
                xres[j] = __builtin_amdgcn_raw_buffer_load_b64(rs, voff, (int)OFF_X1T + so, 0);
                yv[j] = __builtin_amdgcn_raw_buffer_load_b64(rs, voff, (int)OFF_YT + so, 0);
            }
            SB();
        }
        u32x4 B[G2_PH][4];
        auto loadB = [&](int st, int i) {
#pragma unroll
            for (int c = 0; c < 4; ++c) B[st][c] = __builtin_amdgcn_raw_buffer_load_b128(rs, voff, hoffc[c] + kst[i] * 1024, 16);
        };
#pragma unroll
        for (int p = 0; p < G2_PH - 1; ++p) { loadB(p, p); SB(); }
        f32x4 acc[5][4];
        unsigned early = 0;
#pragma unroll
        for (int i = 0; i < 14; ++i) {
            const int st = i % G2_PH;
            if (i + G2_PH - 1 == 5) {
                const char* cb = ws + OFF_CNT + (size_t)(L * 32 + 16 + (rq >> 1)) * 256;
                asm volatile("s_nop 4\n\tglobal_load_dword %0, %1, %2 sc1" : "=v"(early) : "v"(0u), "s"(cb) : "memory");
            }
            if (i + G2_PH - 1 == 8) {
                gu32* c = (gu32*)(ws + OFF_CNT + (size_t)(L * 32 + 16 + (rq >> 1)) * 256);
                unsigned spins = 0;
                asm volatile("s_waitcnt vmcnt(12)" : "+v"(early) :: "memory");
                if (__builtin_amdgcn_readfirstlane(early) < 16u)
                while (__builtin_amdgcn_readfirstlane(__hip_atomic_load(c, __ATOMIC_RELAXED, __HIP_MEMORY_SCOPE_AGENT)) < 16u) {
                    if (++spins > SPIN_CAP) break;
                }
                __builtin_amdgcn_fence(__ATOMIC_ACQUIRE, "wavefront");
                SB();
            }
            if (i + G2_PH - 1 < 14) loadB((i + G2_PH - 1) % G2_PH, i + G2_PH - 1);
            SB();
            if ((i != 7 || wv != 3) && (i != 13 || wv != 2)) {
#pragma unroll
                for (int c = 0; c < 4; ++c)
#pragma unroll
                    for (int j = 0; j < 5; ++j) {
 if (i == 0) asm volatile("v_mfma_f32_16x16x32_bf16 %0, %1, %2, 0" : "=&v"(acc[j][c]) : "a"(A[i][j]), "v"(B[st][c]));
                        else if (i * 5 + j < G2_NAG) asm volatile("v_mfma_f32_16x16x32_bf16 %0, %1, %2, %0" : "+v"(acc[j][c]) : "a"(A[i][j]), "v"(B[st][c]));
                        else                asm volatile("v_mfma_f32_16x16x32_bf16 %0, %1, %2, %0" : "+v"(acc[j][c]) : "v"(A[i][j]), "v"(B[st][c]));
                    }
            }
            SB();
        }
        asm volatile("s_nop 15\n\ts_nop 15" ::: "memory");
        SB();
        STAMP(ws, 2, L * 8 + 6);
        f32x4* red = (f32x4*)(smem + G1_LDS);
        f32x4 o[5];
#pragma unroll
        for (int j = 0; j < 5; ++j) o[j] = acc[j][0];
#pragma unroll
        for (int t = 1; t < 4; ++t) {
            const int dst = (wv + t) & 3;
#pragma unroll
            for (int j = 0; j < 5; ++j) red[(dst * 5 + j) * 64 + lane] = acc[j][t];
            __syncthreads();
#pragma unroll
            for (int j = 0; j < 5; ++j) o[j] += red[(wv * 5 + j) * 64 + lane];
            if (t < 3) __syncthreads();
        }
        const int r = lane & 15;
        const int rb = rq * 4 + wv;
        float x2[5][4];
#pragma unroll
        for (int j = 0; j < 5; ++j) {
            unsigned p01 = pk2(o[j][0], o[j][1]), p23 = pk2(o[j][2], o[j][3]);
            unsigned s01 = pk2(bflo(xres[j].x) + bflo(p01), bfhi(xres[j].x) + bfhi(p01));
            unsigned s23 = pk2(bflo(xres[j].y) + bflo(p23), bfhi(xres[j].y) + bfhi(p23));
            if (L == 1) {
                u32x2 st = {s01, s23};
                *(u32x2*)(out + ((size_t)(rb * 16 + r) * D_ + db_of(cu, j) * 16 + 4 * q) * 2) = st;
            } else {
                x2[j][0] = bflo(s01); x2[j][1] = bfhi(s01); x2[j][2] = bflo(s23); x2[j][3] = bfhi(s23);
            }
        }
        if (L == 0) {
            float s = 0.f;
#pragma unroll
            for (int j = 0; j < 5; ++j) s += (x2[j][0] + x2[j][1]) + (x2[j][2] + x2[j][3]);
            s += __shfl_xor(s, 16); s += __shfl_xor(s, 32);
            const float ml = s * (1.f / 80.f);
            float m2 = 0.f;
#pragma unroll
            for (int j = 0; j < 5; ++j)
#pragma unroll
                for (int e = 0; e < 4; ++e) { float d = x2[j][e] - ml; m2 += d * d; }
            m2 += __shfl_xor(m2, 16); m2 += __shfl_xor(m2, 32);
            gu64* gr = (gu64*)(ws + OFF_GR) + (size_t)rb * 128 + r;
            if (q == 0) {
                unsigned us = __float_as_uint(s), um = __float_as_uint(m2);
                if (us == 0xffffffffu) us = 0x7fc00000u;
                if (um == 0xffffffffu) um = 0x7fc00000u;
                __hip_atomic_store(gr + cu * 16, ((unsigned long long)um << 32) | us, __ATOMIC_RELAXED, __HIP_MEMORY_SCOPE_AGENT);
            }
            unsigned long long g0, g1;
            for (unsigned spins = 0;; ++spins) {
                g0 = __hip_atomic_load(gr + 32 * q, __ATOMIC_RELAXED, __HIP_MEMORY_SCOPE_AGENT);
                g1 = __hip_atomic_load(gr + 32 * q + 16, __ATOMIC_RELAXED, __HIP_MEMORY_SCOPE_AGENT);
                bool ok = ((unsigned)g0 != 0xffffffffu) && ((unsigned)(g0 >> 32) != 0xffffffffu) &&
                          ((unsigned)g1 != 0xffffffffu) && ((unsigned)(g1 >> 32) != 0xffffffffu);
                if (__all(ok) || spins > SPIN_CAP) break;
            }
            STAMP(ws, 2, 7);
            const float s0 = __uint_as_float((unsigned)g0), s1 = __uint_as_float((unsigned)g1);
            float st = s0 + s1;
            st += __shfl_xor(st, 16); st += __shfl_xor(st, 32);
            const float mean = st * (1.f / D_);
            const float d0 = s0 * (1.f / 80.f) - mean, d1 = s1 * (1.f / 80.f) - mean;
            float vt = (__uint_as_float((unsigned)(g0 >> 32)) + __uint_as_float((unsigned)(g1 >> 32))) + 80.f * (d0 * d0 + d1 * d1);
            vt += __shfl_xor(vt, 16); vt += __shfl_xor(vt, 32);
            const float m = rbf(mean);
            const float dn = rbf(sqrtf(rbf(rbf(vt * (1.f / D_)) + 1e-5f)));
            const float rinv = 1.f / dn;
            float x3f[5][4];
#pragma unroll
            for (int j = 0; j < 5; ++j) {
                unsigned a01 = pk2(x2[j][0] - m, x2[j][1] - m), a23 = pk2(x2[j][2] - m, x2[j][3] - m);
                unsigned b01 = pk2(bflo(a01) * rinv, bfhi(a01) * rinv), b23 = pk2(bflo(a23) * rinv, bfhi(a23) * rinv);
                unsigned c01 = pk2(bflo(b01) * bflo(yv[j].x), bfhi(b01) * bfhi(yv[j].x));
                unsigned c23 = pk2(bflo(b23) * bflo(yv[j].y), bfhi(b23) * bfhi(yv[j].y));
                u32x2 x3 = {pk2(x2[j][0] + bflo(c01), x2[j][1] + bfhi(c01)), pk2(x2[j][2] + bflo(c23), x2[j][3] + bfhi(c23))};
                xres[j] = x3;
                if (j & 1) {
                    const int db = db_of(cu, j - 1);
                    u32x4 s4 = {xres[j - 1].x, xres[j - 1].y, x3.x, x3.y};
                    store16_sc1(s4, rs, voff, (int)OFF_X3T + (rb * KS1 + (db >> 1)) * 1024);
                } else if (j == 4) {
                    const int db = db_of(cu, 4);
                    __builtin_amdgcn_raw_buffer_store_b64(x3, rs, voff + (db & 1) * 8, (int)OFF_X3T + (rb * KS1 + (db >> 1)) * 1024, 16);
                }
                x3f[j][0] = bflo(x3.x); x3f[j][1] = bfhi(x3.x); x3f[j][2] = bflo(x3.y); x3f[j][3] = bfhi(x3.y);
            }
            float s3 = 0.f;
#pragma unroll
            for (int j = 0; j < 5; ++j) s3 += (x3f[j][0] + x3f[j][1]) + (x3f[j][2] + x3f[j][3]);
            s3 += __shfl_xor(s3, 16); s3 += __shfl_xor(s3, 32);
            const float ml3 = s3 * (1.f / 80.f);
            float m23 = 0.f;
#pragma unroll
            for (int j = 0; j < 5; ++j)
#pragma unroll
                for (int e = 0; e < 4; ++e) { float d = x3f[j][e] - ml3; m23 += d * d; }
            m23 += __shfl_xor(m23, 16); m23 += __shfl_xor(m23, 32);
            if (q == 0) {
                u32x2 pv = {__float_as_uint(s3), __float_as_uint(m23)};
                __builtin_amdgcn_raw_buffer_store_b64(pv, rs, r * 64 + cu * 8, (int)OFF_GR2 + rb * 1024, 16);
            }
            VMWAIT(0);
            publish_count(ws, 64 + (rq >> 1));
            wait_count(ws, 64 + rp, 16u);
            STAMP(ws, 2, 8);
        }
    }
}

__global__ __launch_bounds__(256, 1) void k2_ffn(char* __restrict__ ws, char* __restrict__ out) {
    extern __shared__ __attribute__((aligned(16))) char smem[];
    STAMP(ws, 2, 0);
    const int b = blockIdx.x;
    const int xcd = b & 7, idx = b >> 3;
    const int rp = 4 * (xcd >> 1) + (idx >> 3), ct = 8 * (xcd & 1) + (idx & 7);
    const int rq = 4 * xcd + (idx >> 3), cu = idx & 7;
    const int lane = threadIdx.x & 63;
    const int wv = __builtin_amdgcn_readfirstlane(threadIdx.x >> 6);
    const __amdgpu_buffer_rsrc_t rs = __builtin_amdgcn_make_buffer_rsrc((void*)ws, 0, (int)WS_USED, 0x00020000);
    u32x2 xres[5];
    SB();
    bf16x8 A[14][5];
    ffn_layer<0>(ws, out, smem, rs, rp, ct, rq, cu, lane, wv, xres, A);
    ffn_layer<1>(ws, out, smem, rs, rp, ct, rq, cu, lane, wv, xres, A);
}

extern "C" void kernel_launch(void* const* d_in, const int* in_sizes, int n_in,
                              void* d_out, int out_size, void* d_ws, size_t ws_size,
                              hipStream_t stream) {
    char* ws = (char*)d_ws;
    hipFuncSetAttribute((const void*)k1_conv, hipFuncAttributeMaxDynamicSharedMemorySize, K1_LDS);
    hipFuncSetAttribute((const void*)k2_ffn, hipFuncAttributeMaxDynamicSharedMemorySize, FFN_LDS);
    k0_prep<<<256, 896, 0, stream>>>((const unsigned*)d_in[0], (const unsigned*)d_in[1], (const unsigned*)d_in[2],
                                     (const u32x4*)d_in[3], ws);
    k1_conv<<<160 + 27 + 30, 512, K1_LDS, stream>>>((const unsigned short*)d_in[4], (const unsigned short*)d_in[5],
                                                    (const unsigned*)d_in[7], (const unsigned*)d_in[8], ws);
    k2_ffn<<<256, 256, FFN_LDS, stream>>>(ws, (char*)d_out);
}
```

```cpp
#define WT1

#include <hip/hip_runtime.h>
#include <hip/hip_bf16.h>
#include <stdint.h>

typedef __attribute__((ext_vector_type(8))) short bf16x8;
typedef __attribute__((ext_vector_type(4))) float f32x4;
typedef __attribute__((ext_vector_type(2))) float f32x2;
typedef __attribute__((ext_vector_type(4))) unsigned int u32x4;
typedef __attribute__((ext_vector_type(2))) unsigned int u32x2;
typedef __attribute__((ext_vector_type(2))) __bf16 bf2_t;

constexpr int S_ = 2048, D_ = 640, F_ = 1728;
constexpr int KS1 = D_ / 32;
constexpr int KS2 = F_ / 32;
constexpr int NB1 = F_ / 16;
constexpr int DB_ = D_ / 16;

constexpr size_t SZ_ACT = (size_t)S_ * D_ * 2;
constexpr size_t OFF_URT = 0;
constexpr size_t OFF_X0T = OFF_URT + SZ_ACT;
constexpr size_t OFF_Q0T = OFF_X0T + SZ_ACT;
constexpr size_t OFF_X1T = OFF_Q0T + SZ_ACT;
constexpr size_t OFF_YT  = OFF_X1T + SZ_ACT;
constexpr size_t OFF_X2T = OFF_YT + SZ_ACT;
constexpr size_t OFF_X3T = OFF_X2T + SZ_ACT;
constexpr size_t OFF_HT  = OFF_X3T + SZ_ACT;
constexpr size_t SZ_HT   = (size_t)S_ * F_ * 2;
constexpr size_t OFF_W1T = OFF_HT + SZ_HT;
constexpr size_t SZ_W    = (size_t)D_ * F_ * 2;
constexpr size_t OFF_W2T = OFF_W1T + SZ_W;
constexpr size_t OFF_CS  = OFF_W2T + SZ_W;
constexpr size_t OFF_HT2 = OFF_CS + 8192;
constexpr size_t OFF_GR  = OFF_HT2 + SZ_HT;
constexpr size_t OFF_GR2 = OFF_GR + (size_t)S_ * 8 * 8;
constexpr size_t OFF_CNT = OFF_GR2 + (size_t)S_ * 8 * 8;
constexpr size_t WS_USED = OFF_CNT + 5 * 16 * 256;

#define STAMP_DECL
#define STAMP(ws_, kid, i) do {} while (0)
#define STAMP_FLUSH(ws_, kid) do {} while (0)
#define STAMP_END(ws_, kid, i) do {} while (0)
#define SB() __builtin_amdgcn_sched_barrier(0)

__device__ __forceinline__ float bflo(unsigned x) { return __uint_as_float(x << 16); }
__device__ __forceinline__ float bfhi(unsigned x) { return __uint_as_float(x & 0xffff0000u); }
__device__ __forceinline__ unsigned pk2(float lo, float hi) {
    f32x2 v = {lo, hi};
    bf2_t r = __builtin_convertvector(v, bf2_t);
    return __builtin_bit_cast(unsigned, r);
}
__device__ __forceinline__ float rbf(float x) { return bflo(pk2(x, 0.f)); }

__device__ __forceinline__ float rowsum4(float x) {
    unsigned u = __float_as_uint(x);
    auto a = __builtin_amdgcn_permlane16_swap(u, u, false, false);
    float y = __uint_as_float(a[0]) + __uint_as_float(a[1]);
    unsigned v = __float_as_uint(y);
    auto c = __builtin_amdgcn_permlane32_swap(v, v, false, false);
    return __uint_as_float(c[0]) + __uint_as_float(c[1]);
}
__device__ __forceinline__ void gst16_wt(void* p, u32x4 v) {
    asm volatile("global_store_dwordx4 %0, %1, off sc1\n\ts_nop 1" :: "v"(p), "v"(v) : "memory");
}
__device__ __forceinline__ void gst8_wt(void* p, u32x2 v) {
    asm volatile("global_store_dwordx2 %0, %1, off sc1" :: "v"(p), "v"(v) : "memory");
}

__global__ __launch_bounds__(896) void k0_prep(const unsigned* __restrict__ q, const unsigned* __restrict__ k,
                                               const unsigned* __restrict__ v, const u32x4* __restrict__ shk,
                                               const u32x4* __restrict__ ssmk, const u32x4* __restrict__ w1,
                                               const u32x4* __restrict__ w2, char* __restrict__ ws) {
    __shared__ __attribute__((aligned(16))) char xs[160 * 8 * 16];
    const int t = threadIdx.x;
    const int b = (blockIdx.x & 7) * 32 + (blockIdx.x >> 3);
    const int s0 = b * 8;
    STAMP_DECL;
    STAMP(ws, 0, 0);
    if (t < 640) {
        const int cp = t % 320, rh = t / 320;
        const int sb = s0 + 4 * rh;
        unsigned kr[7], vr[4];
#pragma unroll
        for (int i = 0; i < 7; ++i) {
            int s = sb - 3 + i;
            kr[i] = k[(size_t)(s > 0 ? s : 0) * (D_ / 2) + cp];
        }
#pragma unroll
        for (int i = 0; i < 4; ++i) vr[i] = v[(size_t)(sb + i) * (D_ / 2) + cp];
        const u32x4 w = shk[cp];
        u32x4 pf0 = w;
        u32x4 pf1 = pf0, pf2 = pf0;
        SB();
#pragma unroll
        for (int i = 0; i < 3; ++i) kr[i] = (sb - 3 + i >= 0) ? kr[i] : 0u;
        const float wa[4] = {bflo(w.x), bfhi(w.x), bflo(w.y), bfhi(w.y)};
        const float wb[4] = {bflo(w.z), bfhi(w.z), bflo(w.w), bfhi(w.w)};
        float ua[4], ub[4];
#pragma unroll
        for (int i = 0; i < 4; ++i) {
            unsigned acc = pk2(bflo(kr[i]) * wa[3], bfhi(kr[i]) * wb[3]);
#pragma unroll
            for (int j = 1; j < 4; ++j) {
                unsigned pr = pk2(bflo(kr[i + j]) * wa[3 - j], bfhi(kr[i + j]) * wb[3 - j]);
                acc = pk2(bflo(acc) + bflo(pr), bfhi(acc) + bfhi(pr));
            }
            ua[i] = bflo(acc) * bflo(vr[i]);
            ub[i] = bfhi(acc) * bfhi(vr[i]);
        }
        u32x2 oa = {pk2(ua[3], ua[2]), pk2(ua[1], ua[0])};
        u32x2 ob = {pk2(ub[3], ub[2]), pk2(ub[1], ub[0])};
        const int d = 2 * cp;
        char* dst = ws + OFF_URT + ((((size_t)(d >> 2) * 256 + (255 - b)) * 4 + (d & 3)) * 16) + (1 - rh) * 8;
        *(u32x2*)dst = oa;
        *(u32x2*)(dst + 16) = ob;
        asm volatile("" :: "v"(pf0), "v"(pf1), "v"(pf2));
    } else {
        const int tt = t - 640;
        if (tt < 64) ((u32x2*)(ws + OFF_GR))[(size_t)s0 * 8 + tt] = (u32x2){0xffffffffu, 0xffffffffu};
        if (b == 0 && tt >= 64 && tt < 64 + 80) ((unsigned*)(ws + OFF_CNT))[(tt - 64) * 64] = 0u;
        const int row = tt >> 5, ll = tt & 31;
        const int s = s0 + row;
        u32x2 xq[5];
#pragma unroll
        for (int i = 0; i < 5; ++i) xq[i] = *(const u32x2*)(q + (size_t)s * (D_ / 2) + 2 * (ll + 32 * i));
        float xv[5][4];
        float sum = 0.f;
#pragma unroll
        for (int i = 0; i < 5; ++i) {
            xv[i][0] = bflo(xq[i].x); xv[i][1] = bfhi(xq[i].x); xv[i][2] = bflo(xq[i].y); xv[i][3] = bfhi(xq[i].y);
            sum += (xv[i][0] + xv[i][1]) + (xv[i][2] + xv[i][3]);
        }
#pragma unroll
        for (int o = 16; o; o >>= 1) sum += __shfl_xor(sum, o, 32);
        const float mean = sum * (1.f / D_);
        float vs = 0.f;
#pragma unroll
        for (int i = 0; i < 5; ++i)
#pragma unroll
            for (int e = 0; e < 4; ++e) { float a = xv[i][e] - mean; vs += a * a; }
#pragma unroll
        for (int o = 16; o; o >>= 1) vs += __shfl_xor(vs, o, 32);
        const float m = rbf(mean);
        const float dn = rbf(sqrtf(rbf(rbf(vs * (1.f / D_)) + 1e-5f)));
        const float rinv = 1.f / dn;
#pragma unroll
        for (int i = 0; i < 5; ++i) {
            const int g = ll + 32 * i;
            unsigned c01 = pk2(xv[i][0] - m, xv[i][1] - m), c23 = pk2(xv[i][2] - m, xv[i][3] - m);
            u32x2 o = {pk2(bflo(c01) * rinv, bfhi(c01) * rinv), pk2(bflo(c23) * rinv, bfhi(c23) * rinv)};
            *(u32x4*)(xs + (g * 8 + row) * 16) = (u32x4){xq[i].x, xq[i].y, o.x, o.y};
        }
    }
    __syncthreads();
    if (t < 640) {
#pragma unroll
        for (int e = 0; e < 2; ++e) {
            const int c = t + 640 * e;
            *(u32x4*)(ws + OFF_X0T + ((size_t)(c >> 3) * S_ + s0 + (c & 7)) * 16) = *(const u32x4*)(xs + c * 16);
        }
    }
    STAMP(ws, 0, 1);
    STAMP_END(ws, 0, 2);
    STAMP_FLUSH(ws, 0);
}

constexpr int HC_STRIDE = 4160;
constexpr int CH_H = 4 * HC_STRIDE;
constexpr int UR_BYTES = 4608;
constexpr int CH_BYTES = CH_H + UR_BYTES;
constexpr int K1_RED_OFF = 4 * CH_BYTES;
constexpr int K1_LDS = K1_RED_OFF + 8 * 8192;

__device__ __forceinline__ void retile_block(const unsigned* __restrict__ W, int ncols, int nks, int cg, int u0, int u1,
                                             char* __restrict__ dstT, float* __restrict__ cs, float* lds) {
    const int t = threadIdx.x, lane = t & 63, wv = t >> 6;
    const int cp = lane & 31, hh = lane >> 5;
    const int n0 = cg * 64 + 2 * cp;
    float sa = 0.f, sb = 0.f;
    unsigned val[5][8];
#pragma unroll
    for (int i = 0; i < 5; ++i) {
        int u = u0 + wv * 10 + 2 * i + hh;
        int uu = u < u1 ? u : u1 - 1;
        int ks = uu >> 2, q = uu & 3;
#pragma unroll
        for (int j = 0; j < 8; ++j) {
            int kk = 32 * ks + (j >> 2) * 16 + 4 * q + (j & 3);
            val[i][j] = W[(size_t)kk * (ncols / 2) + (n0 >> 1)];
        }
    }
#pragma unroll
    for (int i = 0; i < 5; ++i) {
        int u = u0 + wv * 10 + 2 * i + hh;
        if (u < u1) {
            u32x4 lo, hi;
            lo.x = __builtin_amdgcn_perm(val[i][1], val[i][0], 0x05040100u);
            lo.y = __builtin_amdgcn_perm(val[i][3], val[i][2], 0x05040100u);
            lo.z = __builtin_amdgcn_perm(val[i][5], val[i][4], 0x05040100u);
            lo.w = __builtin_amdgcn_perm(val[i][7], val[i][6], 0x05040100u);
            hi.x = __builtin_amdgcn_perm(val[i][1], val[i][0], 0x07060302u);
            hi.y = __builtin_amdgcn_perm(val[i][3], val[i][2], 0x07060302u);
            hi.z = __builtin_amdgcn_perm(val[i][5], val[i][4], 0x07060302u);
            hi.w = __builtin_amdgcn_perm(val[i][7], val[i][6], 0x07060302u);
            u32x4* stg = (u32x4*)(lds + 2048) + (wv * 5 + i) * 128;
            stg[hh * 64 + 2 * cp] = lo;
            stg[hh * 64 + 2 * cp + 1] = hi;
#pragma unroll
            for (int j = 0; j < 8; ++j) { sa += bflo(val[i][j]); sb += bfhi(val[i][j]); }
        }
    }
    asm volatile("s_waitcnt lgkmcnt(0)" ::: "memory");
#pragma unroll
    for (int i = 0; i < 5; ++i)
#pragma unroll
        for (int e = 0; e < 2; ++e) {
            const int u = u0 + wv * 10 + 2 * i + e;
            if (u < u1) {
                const u32x4 vv = ((const u32x4*)(lds + 2048))[(wv * 5 + i) * 128 + e * 64 + lane];
                const int n = cg * 64 + lane;
                gst16_wt(dstT + (((size_t)(n >> 4) * nks + (u >> 2)) * 64 + (u & 3) * 16 + (n & 15)) * 16, vv);
            }
        }
    if (cs) {
        lds[(wv * 2 + hh) * 64 + 2 * cp] = sa;
        lds[(wv * 2 + hh) * 64 + 2 * cp + 1] = sb;
        __syncthreads();
        if (t < 64) {
            float s = 0.f;
#pragma unroll
            for (int i = 0; i < 16; ++i) s += lds[i * 64 + t];
            cs[cg * 64 + t] = s;
        }
    }
}

__global__ __launch_bounds__(512) void k1_conv(const unsigned short* __restrict__ ssmk, const unsigned short* __restrict__ ssmD,
                                               const unsigned* __restrict__ w1, const unsigned* __restrict__ w2,
                                               char* __restrict__ ws) {
    extern __shared__ __attribute__((aligned(16))) char smem[];
    const int b = blockIdx.x, t = threadIdx.x;
    if (b >= 160) {
        STAMP_DECL;
        STAMP(ws, 7, 0);
        int r = b - 160;
        if (r < 27) {
            retile_block(w1, F_, KS1, r, 0, 80, ws + OFF_W1T, (float*)(ws + OFF_CS), (float*)smem);
        } else {
            r -= 27;
            int cg = r / 3, part = r % 3;
            int u0 = part * 80, u1 = (u0 + 80 < 216) ? u0 + 80 : 216;
            retile_block(w2, D_, KS2, cg, u0, u1, ws + OFF_W2T, nullptr, (float*)smem);
        }
        STAMP_END(ws, 7, 1);
        STAMP_FLUSH(ws, 7);
        return;
    }
    STAMP_DECL;
    STAMP(ws, 1, 0);
    const int d0 = 4 * b;
    const int lane = t & 63, wv = __builtin_amdgcn_readfirstlane(t >> 6);
    {
        const u32x4* src = (const u32x4*)(ws + OFF_URT) + (size_t)b * 1024;
        const int hch = t >> 7, hi = t & 127;
        const unsigned short* hp = ssmk + (size_t)(d0 + hch) * S_;
        u32x4 ha[2]; u32x2 hb[2];
#pragma unroll
        for (int e = 0; e < 2; ++e) {
            const int m = hi + 128 * e;
            ha[e] = *(const u32x4*)(hp + 8 * m);
            hb[e] = *(const u32x2*)(hp + (m < 255 ? 8 * m + 8 : 0));
        }
        u32x4 a0 = src[t], a1 = src[t + 512];
        SB();
        char* cbase = smem + hch * CH_BYTES;
        if (hi == 0) {
            u32x4 z = {0u, 0u, 0u, 0u};
            u32x4 c1 = z, c2 = z, c3 = z;
            c1.w = ha[0].x << 16;
            c2.w = ha[0].x;
            c3.z = ha[0].x << 16; c3.w = __builtin_amdgcn_alignbit(ha[0].y, ha[0].x, 16);
            *(u32x4*)(cbase + 16) = z;
            *(u32x4*)(cbase + 16 + HC_STRIDE) = c1;
            *(u32x4*)(cbase + 16 + 2 * HC_STRIDE) = c2;
            *(u32x4*)(cbase + 16 + 3 * HC_STRIDE) = c3;
        } else if (hi < 5) {
            *(u32x4*)(cbase + (hi - 1) * HC_STRIDE) = (u32x4){0u, 0u, 0u, 0u};
        } else if (hi < 37) {
            *(u32x4*)(cbase + CH_H + 4096 + (hi - 5) * 16) = (u32x4){0u, 0u, 0u, 0u};
        }
#pragma unroll
        for (int e = 0; e < 2; ++e) {
            char* base = cbase + (hi + 128 * e + 2) * 16;
            u32x4 A = ha[e]; u32x2 B = hb[e];
            u32x4 c1 = {__builtin_amdgcn_alignbit(A.y, A.x, 16), __builtin_amdgcn_alignbit(A.z, A.y, 16),
                        __builtin_amdgcn_alignbit(A.w, A.z, 16), __builtin_amdgcn_alignbit(B.x, A.w, 16)};
            u32x4 c2 = {A.y, A.z, A.w, B.x};
            u32x4 c3 = {c1.y, c1.z, c1.w, __builtin_amdgcn_alignbit(B.y, B.x, 16)};
            *(u32x4*)(base) = A;
            *(u32x4*)(base + HC_STRIDE) = c1;
            *(u32x4*)(base + 2 * HC_STRIDE) = c2;
            *(u32x4*)(base + 3 * HC_STRIDE) = c3;
        }
        *(u32x4*)(smem + (t & 3) * CH_BYTES + CH_H + (t >> 2) * 16) = a0;
        *(u32x4*)(smem + (t & 3) * CH_BYTES + CH_H + ((t + 512) >> 2) * 16) = a1;
    }
    const int eks = d0 >> 5, ehalf = (d0 >> 4) & 1, eq = (d0 >> 2) & 3;
    u32x4 xqr[4];
#pragma unroll
    for (int i = 0; i < 4; ++i) xqr[i] = *(const u32x4*)(ws + OFF_X0T + ((size_t)b * S_ + t + 512 * i) * 16);
    const u32x2 dsk = *(const u32x2*)(ssmD + d0);
    SB();
    __syncthreads();
    STAMP(ws, 1, 1);
    {
        const int ch = wv & 3, bh = wv >> 2;
        const int jr = lane & 15, q = lane >> 4;
        const unsigned sbase = (unsigned)(size_t)(__attribute__((address_space(3))) char*)smem;
        const unsigned abase = sbase + ch * CH_BYTES + ((jr + 1) & 3) * HC_STRIDE + 16 * q + 2 * ((jr + 1) & ~3) + 64 * (4 * bh);
        const unsigned bbase = sbase + ch * CH_BYTES + CH_H + 2 * (2032 - 16 * jr + 8 * q) + 64 * (4 * bh) - 512 * 7;
        f32x4 acc[8];
#pragma unroll
        for (int c = 0; c < 8; ++c) acc[c] = (f32x4){0.f, 0.f, 0.f, 0.f};
        u32x2 Al[2][8], Ah[2][8];
        bf16x8 Bf[2][8];
#define K1_RA(st, a_, bb_)                                                                                      \
        asm volatile("ds_read_b64 %0, %1 offset:%2" : "=v"(Al[st][a_]) : "v"(abase), "i"(64 * (bb_) + 512 * (a_)) : "memory"); \
        asm volatile("ds_read_b64 %0, %1 offset:%2" : "=v"(Ah[st][a_]) : "v"(abase), "i"(64 * (bb_) + 512 * (a_) + 8) : "memory");
#define K1_RB(st, m_, bb_)                                                                                      \
        asm volatile("ds_read_b128 %0, %1 offset:%2" : "=v"(Bf[st][m_]) : "v"(bbase), "i"(64 * (bb_) + 512 * (7 - (m_))) : "memory");
#define K1_WAIT(st)                                                                                             \
        asm volatile("s_waitcnt lgkmcnt(0)"                                                                     \
                     : "+v"(Al[st][0]), "+v"(Al[st][1]), "+v"(Al[st][2]), "+v"(Al[st][3]), "+v"(Al[st][4]), "+v"(Al[st][5]), "+v"(Al[st][6]), "+v"(Al[st][7]), \
                       "+v"(Ah[st][0]), "+v"(Ah[st][1]), "+v"(Ah[st][2]), "+v"(Ah[st][3]), "+v"(Ah[st][4]), "+v"(Ah[st][5]), "+v"(Ah[st][6]), "+v"(Ah[st][7]) \
                     :: "memory");                                                                              \
        asm volatile(""                                                                                         \
                     : "+v"(Bf[st][0]), "+v"(Bf[st][1]), "+v"(Bf[st][2]), "+v"(Bf[st][3]), "+v"(Bf[st][4]), "+v"(Bf[st][5]), "+v"(Bf[st][6]), "+v"(Bf[st][7]) \
                     :: "memory");
#define K1_MROW(st, a_)                                                                                         \
        {                                                                                                       \
            u32x4 x_ = {Al[st][a_].x, Al[st][a_].y, Ah[st][a_].x, Ah[st][a_].y};                                \
            bf16x8 af_ = __builtin_bit_cast(bf16x8, x_);                                                        \
            _Pragma("unroll") for (int m = 0; m < 8 - (a_); ++m)                                                \
                acc[(a_) + m] = __builtin_amdgcn_mfma_f32_16x16x32_bf16(af_, Bf[st][m], acc[(a_) + m], 0, 0, 0); \
            SB();                                                                                               \
        }
#define K1_ITER(st, bb_, NEXT)                                                                                  \
        K1_WAIT(st)                                                                                             \
        K1_MROW(st, 0) if (NEXT) { K1_RB((st) ^ 1, 0, (bb_) + 1) K1_RB((st) ^ 1, 1, (bb_) + 1) K1_RB((st) ^ 1, 2, (bb_) + 1) K1_RB((st) ^ 1, 3, (bb_) + 1) K1_RA((st) ^ 1, 0, (bb_) + 1) } SB(); \
        K1_MROW(st, 1) if (NEXT) { K1_RB((st) ^ 1, 4, (bb_) + 1) K1_RB((st) ^ 1, 5, (bb_) + 1) K1_RB((st) ^ 1, 6, (bb_) + 1) K1_RB((st) ^ 1, 7, (bb_) + 1) K1_RA((st) ^ 1, 1, (bb_) + 1) } SB(); \
        K1_MROW(st, 2) if (NEXT) { K1_RA((st) ^ 1, 2, (bb_) + 1) K1_RA((st) ^ 1, 3, (bb_) + 1) } SB();          \
        K1_MROW(st, 3) if (NEXT) { K1_RA((st) ^ 1, 4, (bb_) + 1) K1_RA((st) ^ 1, 5, (bb_) + 1) } SB();          \
        K1_MROW(st, 4) if (NEXT) { K1_RA((st) ^ 1, 6, (bb_) + 1) K1_RA((st) ^ 1, 7, (bb_) + 1) } SB();          \
        K1_MROW(st, 5) K1_MROW(st, 6) K1_MROW(st, 7)
        K1_RB(0, 0, 0) K1_RA(0, 0, 0) K1_RB(0, 1, 0) K1_RB(0, 2, 0) K1_RB(0, 3, 0) K1_RB(0, 4, 0) K1_RB(0, 5, 0) K1_RB(0, 6, 0) K1_RB(0, 7, 0)
        K1_RA(0, 1, 0) K1_RA(0, 2, 0) K1_RA(0, 3, 0) K1_RA(0, 4, 0) K1_RA(0, 5, 0) K1_RA(0, 6, 0) K1_RA(0, 7, 0)
        K1_ITER(0, 0, true)
        K1_ITER(1, 1, true)
        K1_ITER(0, 2, true)
        K1_ITER(1, 3, false)
        float* red = (float*)(smem + K1_RED_OFF) + (ch * 2 + bh) * 2048;
#pragma unroll
        for (int c = 0; c < 8; ++c) *(f32x4*)(red + 256 * c + 16 * jr + 4 * (q ^ ((jr >> 1) & 3))) = acc[c];
    }
    __syncthreads();
    STAMP(ws, 1, 2);
    {
        const float* red = (const float*)(smem + K1_RED_OFF);
        float dk[4] = {bflo(dsk.x), bfhi(dsk.x), bflo(dsk.y), bfhi(dsk.y)};
#pragma unroll
        for (int i = 0; i < 4; ++i) {
            int s = t + 512 * i;
            const int sr = s ^ (((s >> 5) & 3) << 2);
            float xs[4] = {bflo(xqr[i].x), bfhi(xqr[i].x), bflo(xqr[i].y), bfhi(xqr[i].y)};
            float qs[4] = {bflo(xqr[i].z), bfhi(xqr[i].z), bflo(xqr[i].w), bfhi(xqr[i].w)};
            float yo[4], xo[4];
#pragma unroll
            for (int c = 0; c < 4; ++c) {
                float y = rbf(red[(c * 2) * 2048 + sr] + red[(c * 2 + 1) * 2048 + sr]);
                unsigned short uu = *(const unsigned short*)(smem + c * CH_BYTES + CH_H + 2 * (2047 - s));
                float u = bflo(uu);
                y = rbf(y + rbf(u * dk[c]));
                yo[c] = y;
                xo[c] = xs[c] + rbf(qs[c] * y);
            }
            size_t off = (((size_t)(s >> 4) * KS1 + eks) * 2 + ehalf) * 512 + (eq * 16 + (s & 15)) * 8;
            u32x2 oy = {pk2(yo[0], yo[1]), pk2(yo[2], yo[3])};
            u32x2 ox = {pk2(xo[0], xo[1]), pk2(xo[2], xo[3])};
            *(u32x2*)(ws + OFF_YT + off) = oy;
            *(u32x2*)(ws + OFF_X1T + off) = ox;
        }
    }
    STAMP(ws, 1, 3);
    STAMP_END(ws, 1, 4);
    STAMP_FLUSH(ws, 1);
}

constexpr int G1_LDS = KS1 * 7 * 1024;
constexpr int FFN_LDS = G1_LDS + 20480;
constexpr unsigned SPIN_CAP = 1u << 22;
typedef __attribute__((address_space(1))) unsigned gu32;
typedef __attribute__((address_space(1))) unsigned long long gu64;

#define GLOAD_X4(dst, voff, sbase, imm) \
    asm volatile("global_load_dwordx4 %0, %1, %2 offset:%3 sc1" : "=v"(dst) : "v"(voff), "s"(sbase), "i"(imm) : "memory")
#define DSR(dst, addr, off) asm volatile("ds_read_b128 %0, %1 offset:%2" : "=v"(dst) : "v"(addr), "i"(off) : "memory")
__device__ __forceinline__ void store16_sc1(u32x4 v, const __amdgpu_buffer_rsrc_t rs, unsigned voff, int soff) {
    __builtin_amdgcn_raw_buffer_store_b128(v, rs, voff, soff, 16);
    asm volatile("s_nop 1" :: "v"(v) : "memory");
}
#define VMWAIT(n) asm volatile("s_waitcnt vmcnt(" #n ")" ::: "memory")

__device__ __forceinline__ void wait_count(char* ws, int idx, unsigned want) {
    if (threadIdx.x == 0) {
        gu32* c = (gu32*)(ws + OFF_CNT + (size_t)idx * 256);
        unsigned spins = 0;
        while (__hip_atomic_load(c, __ATOMIC_RELAXED, __HIP_MEMORY_SCOPE_AGENT) < want) {
            if (++spins > SPIN_CAP) break;
        }
        __builtin_amdgcn_fence(__ATOMIC_ACQUIRE, "wavefront");
    }
    __builtin_amdgcn_s_barrier();
    asm volatile("" ::: "memory");
}
__device__ __forceinline__ void publish_count(char* ws, int idx) {
    __builtin_amdgcn_s_barrier();
    if (threadIdx.x == 0)
        __hip_atomic_fetch_add((gu32*)(ws + OFF_CNT + (size_t)idx * 256), 1u, __ATOMIC_RELAXED, __HIP_MEMORY_SCOPE_AGENT);
}

constexpr int G2_PH = 4;
constexpr int G2_NAG = 52;
constexpr int G2_KEEP = 9;
__device__ __forceinline__ int nb_of(int ct, int i) { int j = i + (ct & 1); j = j >= 7 ? j - 7 : j; return 7 * ct + j; }
__device__ __forceinline__ int db_of(int cu, int j) { int t = j + (cu & 1); t = t >= 5 ? t - 5 : t; return 5 * cu + t; }

__device__ __forceinline__ bf16x8 xjoin(u32x2 a, u32x2 b) { u32x4 t = {a.x, a.y, b.x, b.y}; return __builtin_bit_cast(bf16x8, t); }
__device__ __forceinline__ int ksA_of(int a) { return 7 * (a >> 2) + (a & 1) + ((a & 2) << 1); }
__device__ __forceinline__ int ksB_of(int b) { int g = b / 3, r = b - 3 * g; return 7 * g + (r == 2 ? 6 : r + 2); }

template <int L>
__device__ __forceinline__ void ffn_layer(char* __restrict__ ws, char* __restrict__ out, char* smem, const __amdgpu_buffer_rsrc_t rs,
                                          const int rp, const int ct, const int rq, const int cu, const int lane, const int wv,
                                          u32x2 (&xres)[5], bf16x8 (&A)[14][5]
                                          ) {
    const int rb0 = rp * 8 + 2 * wv;
    const int q = lane >> 4;
    const unsigned voff = lane * 16;
    int kst[14];
#pragma unroll
    for (int i = 0; i < 8; ++i) { int a = 8 * wv + i; kst[i] = ksA_of(a < 31 ? a : 30); }
#pragma unroll
    for (int i = 0; i < 6; ++i) { int bb = (wv < 3 ? 6 * wv : 17) + i; if (wv == 2 && i == 5) bb = 16; kst[8 + i] = ksB_of(bb); }
    int wsoff[5];
#pragma unroll
    for (int j = 0; j < 5; ++j) wsoff[j] = db_of(cu, j) * KS2 * 1024;
    const char* w2base = ws + OFF_W2T;
    unsigned voffA[5];
#pragma unroll
    for (int j = 0; j < 5; ++j) voffA[j] = voff + wsoff[j];
    auto loadA1 = [&](int f) {
        const int g = f / 5, j = f % 5;
        if (f < 70 && (L == 0 || g >= G2_KEEP)) {
            const char* pb = w2base + kst[g] * 1024;
            if (f < G2_NAG) asm volatile("global_load_dwordx4 %0, %1, %2" : "=a"(A[g][j]) : "v"(voffA[j]), "s"(pb) : "memory");
            else A[g][j] = __builtin_bit_cast(bf16x8, __builtin_amdgcn_raw_buffer_load_b128(rs, voff, (int)OFF_W2T + wsoff[j] + kst[g] * 1024, 0));
        }
    };
    constexpr int F0 = (L == 0) ? 0 : 5 * G2_KEEP;
    const int hoff = (int)(L == 0 ? OFF_HT : OFF_HT2);
    {
        const char* xbase = ws + (L == 0 ? OFF_X1T : OFF_X3T) + (size_t)rb0 * (KS1 * 1024);
        const char* W1T = ws + OFF_W1T;
        f32x4 csv[7];
#pragma unroll
        for (int i = 0; i < 7; ++i) {
            int nb = nb_of(ct, i); nb = nb < NB1 ? nb : NB1 - 1;
            const char* cb = ws + OFF_CS + nb * 64;
            asm volatile("s_nop 4\n\tglobal_load_dwordx4 %0, %1, %2" : "=v"(csv[i]) : "v"(q * 16), "s"(cb) : "memory");
        }
        SB();
        bf16x8 xf[2][KS1];
        u32x4 pst[2];
        const unsigned voffB = voff + KS1 * 1024;
        const unsigned lds0 = (unsigned)(size_t)(__attribute__((address_space(3))) char*)smem;
        u32x2 xlo[2][KS1], xhi[2][KS1];
#define GLOAD_X2(dst, voff, sbase, imm) \
    asm volatile("global_load_dwordx2 %0, %1, %2 offset:%3" : "=v"(dst) : "v"(voff), "s"(sbase), "i"(imm) : "memory")
#define LX(r, ks) { if (L == 0) { unsigned v_ = lane * 8 + ((r) ? KS1 * 1024 : 0) + ((ks) >> 2) * 4096;                    \
                        GLOAD_X2(xlo[r][ks], v_, xbase, ((ks) & 3) * 1024); GLOAD_X2(xhi[r][ks], v_, xbase, ((ks) & 3) * 1024 + 512); } \
                    else { unsigned v_ = ((r) ? voffB : voff) + ((ks) >> 2) * 4096; GLOAD_X4(xf[r][ks], v_, xbase, ((ks) & 3) * 1024); } }
#define XJOIN(r, ks) xf[r][ks] = xjoin(xlo[r][ks], xhi[r][ks]);
#define WAIT_X0(c, n)                                                                                           \
    asm volatile("s_waitcnt vmcnt(" #n ")"                                                                      \
                 : "+v"(xlo[0][4 * (c)]), "+v"(xlo[0][4 * (c) + 1]), "+v"(xlo[0][4 * (c) + 2]), "+v"(xlo[0][4 * (c) + 3]), \
                   "+v"(xlo[1][4 * (c)]), "+v"(xlo[1][4 * (c) + 1]), "+v"(xlo[1][4 * (c) + 2]), "+v"(xlo[1][4 * (c) + 3]), \
                   "+v"(xhi[0][4 * (c)]), "+v"(xhi[0][4 * (c) + 1]), "+v"(xhi[0][4 * (c) + 2]), "+v"(xhi[0][4 * (c) + 3]), \
                   "+v"(xhi[1][4 * (c)]), "+v"(xhi[1][4 * (c) + 1]), "+v"(xhi[1][4 * (c) + 2]), "+v"(xhi[1][4 * (c) + 3])  \
                 :: "memory");                                                                                  \
    XJOIN(0, 4 * (c)) XJOIN(0, 4 * (c) + 1) XJOIN(0, 4 * (c) + 2) XJOIN(0, 4 * (c) + 3)                         \
    XJOIN(1, 4 * (c)) XJOIN(1, 4 * (c) + 1) XJOIN(1, 4 * (c) + 2) XJOIN(1, 4 * (c) + 3)
#define LW(c, i)                                                                                                \
    {                                                                                                           \
        int nb = nb_of(ct, i); nb = nb < NB1 ? nb : NB1 - 1;                                                    \
        int ks_ = 4 * (c) + wv;                                                                                 \
        const char* gp_ = W1T + ((size_t)nb * KS1 + ks_) * 1024;                                                \
        asm volatile("s_mov_b32 m0, %2\n\ts_nop 0\n\tglobal_load_lds_dwordx4 %0, %1"                            \
                     :: "v"(voff), "s"(gp_), "s"(lds0 + (unsigned)((ks_ * 7 + (i)) * 1024)) : "memory");        \
    }
#define ISSUE_X(c) LX(0, 4 * (c)) LX(1, 4 * (c)) LX(0, 4 * (c) + 1) LX(1, 4 * (c) + 1) LX(0, 4 * (c) + 2) LX(1, 4 * (c) + 2) LX(0, 4 * (c) + 3) LX(1, 4 * (c) + 3)
#define ISSUE_WA(c) LW(c, 0) LW(c, 1) LW(c, 2) LW(c, 3)
#define WAIT_X(c, n)                                                                                           \
    asm volatile("s_waitcnt vmcnt(" #n ")"                                                                      \
                 : "+v"(xf[0][4 * (c)]), "+v"(xf[0][4 * (c) + 1]), "+v"(xf[0][4 * (c) + 2]), "+v"(xf[0][4 * (c) + 3]), \
                   "+v"(xf[1][4 * (c)]), "+v"(xf[1][4 * (c) + 1]), "+v"(xf[1][4 * (c) + 2]), "+v"(xf[1][4 * (c) + 3])  \
                 :: "memory");
#define BARR() __builtin_amdgcn_s_barrier(); asm volatile("" ::: "memory");
#define CSV_LANDED() asm volatile("" : "+v"(csv[0]), "+v"(csv[1]), "+v"(csv[2]), "+v"(csv[3]), "+v"(csv[4]), "+v"(csv[5]), "+v"(csv[6]))
#define WAITL4(w) asm volatile("s_waitcnt lgkmcnt(0)" : "+v"(w[0]), "+v"(w[1]), "+v"(w[2]), "+v"(w[3]) :: "memory")
#define WAITL3(w) asm volatile("s_waitcnt lgkmcnt(0)" : "+v"(w[0]), "+v"(w[1]), "+v"(w[2]) :: "memory")
#define MM2(wf, wi, ks, i)                                                                                      \
    acc[0][i] = __builtin_amdgcn_mfma_f32_16x16x32_bf16(wf[wi], xf[0][ks], acc[0][i], 0, 0, 0);                 \
    acc[1][i] = __builtin_amdgcn_mfma_f32_16x16x32_bf16(wf[wi], xf[1][ks], acc[1][i], 0, 0, 0);
#define LDW1(dst, di, ks, i) { DSR(dst[di], lbase + ((ks) * 7168 / 57344) * 57344, ((ks) * 7168) % 57344 + (i) * 1024); }
#define STEPA(cur, nxt, ks, PRE, S0, S1, S2, S3)                                                                \
    {                                                                                                           \
        PRE                                                                                                     \
        WAITL4(cur);                                                                                            \
        MM2(cur, 0, ks, 0) SB(); if ((ks) < 19) LDW1(nxt, 0, (ks) + 1, 0) else LDW1(nxt, 0, 0, 4) S0 SB();      \
        MM2(cur, 1, ks, 1) SB(); if ((ks) < 19) LDW1(nxt, 1, (ks) + 1, 1) else LDW1(nxt, 1, 0, 5) S1 SB();      \
        MM2(cur, 2, ks, 2) SB(); if ((ks) < 19) LDW1(nxt, 2, (ks) + 1, 2) else LDW1(nxt, 2, 0, 6) S2 SB();      \
        if ((ks) < 19) LDW1(nxt, 3, (ks) + 1, 3) SB();                                                          \
        MM2(cur, 3, ks, 3)                                                                                      \
        if (L == 0) {                                                                                           \
            asum[0] = __builtin_amdgcn_mfma_f32_16x16x32_bf16(ones, xf[0][ks], asum[0], 0, 0, 0);               \
            asum[1] = __builtin_amdgcn_mfma_f32_16x16x32_bf16(ones, xf[1][ks], asum[1], 0, 0, 0);               \
            SB(); S3 SB();                                                                                      \
            asq[0] = __builtin_amdgcn_mfma_f32_16x16x32_bf16(xf[0][ks], xf[0][ks], asq[0], 0, 0, 0);            \
            asq[1] = __builtin_amdgcn_mfma_f32_16x16x32_bf16(xf[1][ks], xf[1][ks], asq[1], 0, 0, 0);            \
            SB();                                                                                               \
        } else { SB(); S3 SB(); }                                                                               \
    }
#define CHUNKA_XW(c, cn, PRE3)                                                                                  \
        STEPA(wA, wB, 4 * (c), , LX(0, 4 * (cn)), LX(1, 4 * (cn)), LX(0, 4 * (cn) + 1), LX(1, 4 * (cn) + 1))    \
        STEPA(wB, wA, 4 * (c) + 1, , LX(0, 4 * (cn) + 2), LX(1, 4 * (cn) + 2), LX(0, 4 * (cn) + 3), LX(1, 4 * (cn) + 3)) \
        STEPA(wA, wB, 4 * (c) + 2, , LW(cn, 0), LW(cn, 1), LW(cn, 2), LW(cn, 3))                                \
        STEPA(wB, wA, 4 * (c) + 3, PRE3, , , , )
#define CHUNKA_X(c, cn, PRE3)                                                                                   \
        STEPA(wA, wB, 4 * (c), , LX(0, 4 * (cn)), LX(1, 4 * (cn)), LX(0, 4 * (cn) + 1), )                       \
        STEPA(wB, wA, 4 * (c) + 1, , LX(1, 4 * (cn) + 1), LX(0, 4 * (cn) + 2), LX(1, 4 * (cn) + 2), )           \
        STEPA(wA, wB, 4 * (c) + 2, , LX(0, 4 * (cn) + 3), LX(1, 4 * (cn) + 3), , )                              \
        STEPA(wB, wA, 4 * (c) + 3, PRE3, , , , )
#define CHUNKA_0(c, PRE3)                                                                                       \
        STEPA(wA, wB, 4 * (c), , , , , )                                                                        \
        STEPA(wB, wA, 4 * (c) + 1, , , , , )                                                                    \
        STEPA(wA, wB, 4 * (c) + 2, , , , , )                                                                    \
        STEPA(wB, wA, 4 * (c) + 3, PRE3, , , , )
        const unsigned lbase = (unsigned)(size_t)(__attribute__((address_space(3))) char*)smem + lane * 16;
        f32x4 acc[2][7];
#pragma unroll
        for (int i = 0; i < 7; ++i) { acc[0][i] = (f32x4){0.f, 0.f, 0.f, 0.f}; acc[1][i] = (f32x4){0.f, 0.f, 0.f, 0.f}; }
        f32x4 asum[2] = {(f32x4){0.f, 0.f, 0.f, 0.f}, (f32x4){0.f, 0.f, 0.f, 0.f}};
        f32x4 asq[2] = {(f32x4){0.f, 0.f, 0.f, 0.f}, (f32x4){0.f, 0.f, 0.f, 0.f}};
        const bf16x8 ones = {0x3f80, 0x3f80, 0x3f80, 0x3f80, 0x3f80, 0x3f80, 0x3f80, 0x3f80};
        float mm[2], ri[2], dnv[2];
        auto row_stats = [&]() {
#pragma unroll
        for (int r = 0; r < 2; ++r) {
            float mean, var;
            if (L == 0) {
                const int tk = lane & 15;
                float dsel = (tk & 2) ? ((tk & 1) ? asq[r][3] : asq[r][2]) : ((tk & 1) ? asq[r][1] : asq[r][0]);
                float bq = rowsum4((tk >> 2) == q ? dsel : 0.f);
                mean = asum[r][0] * (1.f / D_);
                var = fmaxf(bq * (1.f / D_) - mean * mean, 0.f);
            } else {
                const float st = rowsum4(__uint_as_float(pst[r].x) + __uint_as_float(pst[r].z));
                const float vt = rowsum4(__uint_as_float(pst[r].y) + __uint_as_float(pst[r].w));
                mean = st * (1.f / D_);
                var = fmaxf(vt * (1.f / D_) - mean * mean, 0.f);
            }
            mm[r] = rbf(mean);
            dnv[r] = rbf(sqrtf(rbf(rbf(var) + 1e-5f)));
            ri[r] = __builtin_amdgcn_rcpf(dnv[r]);
        }
        };
        bf16x8 wA[4], wB[4];
        asm volatile("s_nop 4" : "+s"(xbase) :: "memory");
        if (L == 0) {
            ISSUE_X(0) ISSUE_WA(0) ISSUE_X(1) ISSUE_WA(1)
            WAIT_X0(0, 20) BARR()
            CSV_LANDED();
            STAMP(ws, 2, 1);
            LDW1(wA, 0, 0, 0) LDW1(wA, 1, 0, 1) LDW1(wA, 2, 0, 2) LDW1(wA, 3, 0, 3)
            CHUNKA_XW(0, 2, WAIT_X0(1, 20) BARR())
            CHUNKA_XW(1, 3, WAIT_X0(2, 20) BARR())
            CHUNKA_XW(2, 4, WAIT_X0(3, 20) BARR())
            STEPA(wA, wB, 12, , LW(0, 4), LW(0, 5), LW(0, 6), LW(1, 4))
            STEPA(wB, wA, 13, , LW(1, 5), LW(1, 6), LW(2, 4), LW(2, 5))
            STEPA(wA, wB, 14, , LW(2, 6), LW(3, 4), LW(3, 5), LW(3, 6))
            STEPA(wB, wA, 15, WAIT_X0(4, 12) BARR(), LW(4, 4), LW(4, 5), LW(4, 6), )
            STEPA(wA, wB, 16, , loadA1(0);, loadA1(1);, loadA1(2);, loadA1(3);)
            STEPA(wB, wA, 17, , loadA1(4);, loadA1(5);, loadA1(6);, loadA1(7);)
            STEPA(wA, wB, 18, , loadA1(8);, loadA1(9);, loadA1(10);, loadA1(11);)
            STEPA(wB, wA, 19, VMWAIT(12); BARR(), loadA1(12);, loadA1(13);, loadA1(14);, loadA1(15);)
        } else {
            ISSUE_X(0) ISSUE_X(1)
            {
                const char* gb = ws + OFF_GR2 + (size_t)rb0 * 1024;
                unsigned vo = (lane & 15) * 64 + q * 16;
                GLOAD_X4(pst[0], vo, gb, 0);
                GLOAD_X4(pst[1], vo, gb, 1024);
            }
            LDW1(wA, 0, 0, 0) LDW1(wA, 1, 0, 1) LDW1(wA, 2, 0, 2) LDW1(wA, 3, 0, 3)
            WAIT_X(0, 10)
            CSV_LANDED();
            STAMP(ws, 2, 9);
            CHUNKA_X(0, 2, WAIT_X(1, 10))
            CHUNKA_X(1, 3, WAIT_X(2, 8))
            asm volatile("" : "+v"(pst[0]), "+v"(pst[1]));
            CHUNKA_X(2, 4, WAIT_X(3, 8))
            STEPA(wA, wB, 12, , , , , )
            STEPA(wB, wA, 13, , row_stats();, , , )
            STEPA(wA, wB, 14, , , , , )
            STEPA(wB, wA, 15, WAIT_X(4, 0), , , , )
            CHUNKA_0(4, )
        }
        STAMP(ws, 2, L * 8 + 2);
        if (L == 0) row_stats();
        SB();
        u32x2 hst[2][7];
        auto epih = [&](int r, int i, int h) {
            const float m = mm[r], dn = dnv[r], rz = ri[r] * -1.44269504089f;
            f32x2 a0 = {acc[r][i][2 * h], acc[r][i][2 * h + 1]};
            f32x2 c0 = {csv[i][2 * h], csv[i][2 * h + 1]};
            f32x2 t0 = a0 - m * c0;
            f32x2 z0 = t0 * rz;
            f32x2 e0 = {__builtin_amdgcn_exp2f(z0.x), __builtin_amdgcn_exp2f(z0.y)};
            e0 = e0 * dn + dn;
            f32x2 r0 = {__builtin_amdgcn_rcpf(e0.x), __builtin_amdgcn_rcpf(e0.y)};
            t0 *= r0;
            const unsigned pv = pk2(t0.x, t0.y);
            if (h == 0) hst[r][i].x = pv; else hst[r][i].y = pv;
        };
        auto epi = [&](int r, int i) { epih(r, i, 0); epih(r, i, 1); };
        auto store_pair = [&](int r, int i) {
            const int nb = nb_of(ct, i);
            u32x4 s4 = {hst[r][i].x, hst[r][i].y, hst[r][i + 1].x, hst[r][i + 1].y};
            const int so = nb < NB1 ? hoff + ((rb0 + r) * KS2 + (nb >> 1)) * 1024 : 0x7ffffc00;
            store16_sc1(s4, rs, voff, so);
        };
#define PIN(x) asm volatile("" : "+v"(x))
#define MM1(wf, wi, r, ks, i) acc[r][i] = __builtin_amdgcn_mfma_f32_16x16x32_bf16(wf[wi], xf[r][ks], acc[r][i], 0, 0, 0); SB();
#define NA_IN(lo, hi) ((3 * (hi) < F0 ? F0 : 3 * (hi)) - (3 * (lo) < F0 ? F0 : 3 * (lo)))
        float tt[32], zz[32];
        const float nm[2] = {-mm[0], -mm[1]};
        const float rzv[2] = {ri[0] * -1.44269504089f, ri[1] * -1.44269504089f};
        bf16x8 wC[3];
        LDW1(wB, 0, 1, 4) LDW1(wB, 1, 1, 5) LDW1(wB, 2, 1, 6)
        asm volatile("s_waitcnt lgkmcnt(3)" : "+v"(wA[0]), "+v"(wA[1]), "+v"(wA[2]) :: "memory");
        MM1(wA, 0, 0, 0, 4) LDW1(wC, 0, 2, 4) tt[0] = fmaf(nm[0], csv[0][0], acc[0][0][0]); PIN(tt[0]); SB();
        MM1(wA, 0, 1, 0, 4)  SB();
        MM1(wA, 1, 0, 0, 5) LDW1(wC, 1, 2, 5) SB();
        MM1(wA, 1, 1, 0, 5) tt[1] = fmaf(nm[0], csv[0][1], acc[0][0][1]); PIN(tt[1]); loadA1(16); SB();
        MM1(wA, 2, 0, 0, 6) LDW1(wC, 2, 2, 6) zz[0] = tt[0] * rzv[0]; PIN(zz[0]); SB();
        MM1(wA, 2, 1, 0, 6) loadA1(17); SB();
        asm volatile("s_waitcnt lgkmcnt(3)" : "+v"(wB[0]), "+v"(wB[1]), "+v"(wB[2]) :: "memory");
        MM1(wB, 0, 0, 1, 4) LDW1(wA, 0, 3, 4) tt[2] = fmaf(nm[0], csv[0][2], acc[0][0][2]); PIN(tt[2]); SB();
        MM1(wB, 0, 1, 1, 4) zz[0] = __builtin_amdgcn_exp2f(zz[0]); PIN(zz[0]);  SB();
        MM1(wB, 1, 0, 1, 5) LDW1(wA, 1, 3, 5) zz[1] = tt[1] * rzv[0]; PIN(zz[1]); SB();
        MM1(wB, 1, 1, 1, 5) tt[3] = fmaf(nm[0], csv[0][3], acc[0][0][3]); PIN(tt[3]); zz[1] = __builtin_amdgcn_exp2f(zz[1]); PIN(zz[1]); loadA1(18); SB();
        MM1(wB, 2, 0, 1, 6) LDW1(wA, 2, 3, 6) zz[2] = tt[2] * rzv[0]; PIN(zz[2]); SB();
        MM1(wB, 2, 1, 1, 6) zz[0] = fmaf(zz[0], dnv[0], dnv[0]); PIN(zz[0]); loadA1(19); SB();
        asm volatile("s_waitcnt lgkmcnt(3)" : "+v"(wC[0]), "+v"(wC[1]), "+v"(wC[2]) :: "memory");
        MM1(wC, 0, 0, 2, 4) LDW1(wB, 0, 4, 4) tt[4] = fmaf(nm[0], csv[1][0], acc[0][1][0]); PIN(tt[4]); zz[2] = __builtin_amdgcn_exp2f(zz[2]); PIN(zz[2]); SB();
        MM1(wC, 0, 1, 2, 4) zz[3] = tt[3] * rzv[0]; PIN(zz[3]); zz[0] = __builtin_amdgcn_rcpf(zz[0]); PIN(zz[0]);  SB();
        MM1(wC, 1, 0, 2, 5) LDW1(wB, 1, 4, 5) zz[1] = fmaf(zz[1], dnv[0], dnv[0]); PIN(zz[1]); SB();
        MM1(wC, 1, 1, 2, 5) tt[5] = fmaf(nm[0], csv[1][1], acc[0][1][1]); PIN(tt[5]); zz[3] = __builtin_amdgcn_exp2f(zz[3]); PIN(zz[3]); loadA1(20); SB();
        MM1(wC, 2, 0, 2, 6) LDW1(wB, 2, 4, 6) zz[4] = tt[4] * rzv[0]; PIN(zz[4]); zz[1] = __builtin_amdgcn_rcpf(zz[1]); PIN(zz[1]); SB();
        MM1(wC, 2, 1, 2, 6) zz[2] = fmaf(zz[2], dnv[0], dnv[0]); PIN(zz[2]); tt[0] = tt[0] * zz[0]; PIN(tt[0]); loadA1(21); SB();
        asm volatile("s_waitcnt lgkmcnt(3)" : "+v"(wA[0]), "+v"(wA[1]), "+v"(wA[2]) :: "memory");
        MM1(wA, 0, 0, 3, 4) LDW1(wC, 0, 5, 4) tt[6] = fmaf(nm[0], csv[1][2], acc[0][1][2]); PIN(tt[6]); zz[4] = __builtin_amdgcn_exp2f(zz[4]); PIN(zz[4]); SB();
        MM1(wA, 0, 1, 3, 4) zz[5] = tt[5] * rzv[0]; PIN(zz[5]); zz[2] = __builtin_amdgcn_rcpf(zz[2]); PIN(zz[2]); loadA1(22); SB();
        MM1(wA, 1, 0, 3, 5) LDW1(wC, 1, 5, 5) zz[3] = fmaf(zz[3], dnv[0], dnv[0]); PIN(zz[3]); tt[1] = tt[1] * zz[1]; PIN(tt[1]); SB();
        MM1(wA, 1, 1, 3, 5) tt[7] = fmaf(nm[0], csv[1][3], acc[0][1][3]); PIN(tt[7]); zz[5] = __builtin_amdgcn_exp2f(zz[5]); PIN(zz[5]); zz[6] = tt[6] * rzv[0]; PIN(zz[6]);  SB();
        MM1(wA, 2, 0, 3, 6) LDW1(wC, 2, 5, 6) zz[3] = __builtin_amdgcn_rcpf(zz[3]); PIN(zz[3]); zz[4] = fmaf(zz[4], dnv[0], dnv[0]); PIN(zz[4]); SB();
        MM1(wA, 2, 1, 3, 6) tt[2] = tt[2] * zz[2]; PIN(tt[2]); hst[0][0].x = pk2(tt[0], tt[1]); loadA1(23); SB();
        asm volatile("s_waitcnt lgkmcnt(3)" : "+v"(wB[0]), "+v"(wB[1]), "+v"(wB[2]) :: "memory");
        MM1(wB, 0, 0, 4, 4) LDW1(wA, 0, 6, 4) tt[8] = fmaf(nm[0], csv[2][0], acc[0][2][0]); PIN(tt[8]); zz[6] = __builtin_amdgcn_exp2f(zz[6]); PIN(zz[6]); SB();
        MM1(wB, 0, 1, 4, 4) zz[7] = tt[7] * rzv[0]; PIN(zz[7]); zz[4] = __builtin_amdgcn_rcpf(zz[4]); PIN(zz[4]); loadA1(24); SB();
        MM1(wB, 1, 0, 4, 5) LDW1(wA, 1, 6, 5) zz[5] = fmaf(zz[5], dnv[0], dnv[0]); PIN(zz[5]); tt[3] = tt[3] * zz[3]; PIN(tt[3]); SB();
        MM1(wB, 1, 1, 4, 5) tt[9] = fmaf(nm[0], csv[2][1], acc[0][2][1]); PIN(tt[9]); zz[7] = __builtin_amdgcn_exp2f(zz[7]); PIN(zz[7]); zz[8] = tt[8] * rzv[0]; PIN(zz[8]);  SB();
        MM1(wB, 2, 0, 4, 6) LDW1(wA, 2, 6, 6) zz[5] = __builtin_amdgcn_rcpf(zz[5]); PIN(zz[5]); zz[6] = fmaf(zz[6], dnv[0], dnv[0]); PIN(zz[6]); SB();
        MM1(wB, 2, 1, 4, 6) tt[4] = tt[4] * zz[4]; PIN(tt[4]); hst[0][0].y = pk2(tt[2], tt[3]); loadA1(25); SB();
        asm volatile("s_waitcnt lgkmcnt(3)" : "+v"(wC[0]), "+v"(wC[1]), "+v"(wC[2]) :: "memory");
        MM1(wC, 0, 0, 5, 4) LDW1(wB, 0, 7, 4) tt[10] = fmaf(nm[0], csv[2][2], acc[0][2][2]); PIN(tt[10]); zz[8] = __builtin_amdgcn_exp2f(zz[8]); PIN(zz[8]); SB();
        MM1(wC, 0, 1, 5, 4) zz[9] = tt[9] * rzv[0]; PIN(zz[9]); zz[6] = __builtin_amdgcn_rcpf(zz[6]); PIN(zz[6]); loadA1(26); SB();
        MM1(wC, 1, 0, 5, 5) LDW1(wB, 1, 7, 5) zz[7] = fmaf(zz[7], dnv[0], dnv[0]); PIN(zz[7]); tt[5] = tt[5] * zz[5]; PIN(tt[5]); SB();
        MM1(wC, 1, 1, 5, 5) tt[11] = fmaf(nm[0], csv[2][3], acc[0][2][3]); PIN(tt[11]); zz[9] = __builtin_amdgcn_exp2f(zz[9]); PIN(zz[9]); zz[10] = tt[10] * rzv[0]; PIN(zz[10]);  SB();
        MM1(wC, 2, 0, 5, 6) LDW1(wB, 2, 7, 6) zz[7] = __builtin_amdgcn_rcpf(zz[7]); PIN(zz[7]); zz[8] = fmaf(zz[8], dnv[0], dnv[0]); PIN(zz[8]); SB();
        MM1(wC, 2, 1, 5, 6) tt[6] = tt[6] * zz[6]; PIN(tt[6]); hst[0][1].x = pk2(tt[4], tt[5]); loadA1(27); SB();
        asm volatile("s_waitcnt lgkmcnt(3)" : "+v"(wA[0]), "+v"(wA[1]), "+v"(wA[2]) :: "memory");
        MM1(wA, 0, 0, 6, 4) LDW1(wC, 0, 8, 4) tt[12] = fmaf(nm[0], csv[3][0], acc[0][3][0]); PIN(tt[12]); zz[10] = __builtin_amdgcn_exp2f(zz[10]); PIN(zz[10]); SB();
        MM1(wA, 0, 1, 6, 4) zz[11] = tt[11] * rzv[0]; PIN(zz[11]); zz[8] = __builtin_amdgcn_rcpf(zz[8]); PIN(zz[8]); loadA1(28); SB();
        MM1(wA, 1, 0, 6, 5) LDW1(wC, 1, 8, 5) zz[9] = fmaf(zz[9], dnv[0], dnv[0]); PIN(zz[9]); tt[7] = tt[7] * zz[7]; PIN(tt[7]); SB();
        MM1(wA, 1, 1, 6, 5) tt[13] = fmaf(nm[0], csv[3][1], acc[0][3][1]); PIN(tt[13]); zz[11] = __builtin_amdgcn_exp2f(zz[11]); PIN(zz[11]); zz[12] = tt[12] * rzv[0]; PIN(zz[12]); loadA1(29); SB();
        MM1(wA, 2, 0, 6, 6) LDW1(wC, 2, 8, 6) zz[9] = __builtin_amdgcn_rcpf(zz[9]); PIN(zz[9]); zz[10] = fmaf(zz[10], dnv[0], dnv[0]); PIN(zz[10]); tt[8] = tt[8] * zz[8]; PIN(tt[8]); SB();
        MM1(wA, 2, 1, 6, 6) hst[0][1].y = pk2(tt[6], tt[7]); store_pair(0, 0);  SB();
        asm volatile("s_waitcnt lgkmcnt(3)" : "+v"(wB[0]), "+v"(wB[1]), "+v"(wB[2]) :: "memory");
        MM1(wB, 0, 0, 7, 4) LDW1(wA, 0, 9, 4) tt[14] = fmaf(nm[0], csv[3][2], acc[0][3][2]); PIN(tt[14]); zz[12] = __builtin_amdgcn_exp2f(zz[12]); PIN(zz[12]); SB();
        MM1(wB, 0, 1, 7, 4) zz[13] = tt[13] * rzv[0]; PIN(zz[13]); zz[10] = __builtin_amdgcn_rcpf(zz[10]); PIN(zz[10]); loadA1(30); SB();
        MM1(wB, 1, 0, 7, 5) LDW1(wA, 1, 9, 5) zz[11] = fmaf(zz[11], dnv[0], dnv[0]); PIN(zz[11]); tt[9] = tt[9] * zz[9]; PIN(tt[9]); SB();
        MM1(wB, 1, 1, 7, 5) tt[15] = fmaf(nm[0], csv[3][3], acc[0][3][3]); PIN(tt[15]); zz[13] = __builtin_amdgcn_exp2f(zz[13]); PIN(zz[13]); zz[14] = tt[14] * rzv[0]; PIN(zz[14]); loadA1(31); SB();
        MM1(wB, 2, 0, 7, 6) LDW1(wA, 2, 9, 6) zz[11] = __builtin_amdgcn_rcpf(zz[11]); PIN(zz[11]); zz[12] = fmaf(zz[12], dnv[0], dnv[0]); PIN(zz[12]); SB();
        MM1(wB, 2, 1, 7, 6) tt[10] = tt[10] * zz[10]; PIN(tt[10]); hst[0][2].x = pk2(tt[8], tt[9]);  SB();
        asm volatile("s_waitcnt lgkmcnt(3)" : "+v"(wC[0]), "+v"(wC[1]), "+v"(wC[2]) :: "memory");
        MM1(wC, 0, 0, 8, 4) LDW1(wB, 0, 10, 4) tt[16] = fmaf(nm[1], csv[0][0], acc[1][0][0]); PIN(tt[16]); zz[14] = __builtin_amdgcn_exp2f(zz[14]); PIN(zz[14]); SB();
        MM1(wC, 0, 1, 8, 4) zz[15] = tt[15] * rzv[0]; PIN(zz[15]); zz[12] = __builtin_amdgcn_rcpf(zz[12]); PIN(zz[12]); loadA1(32); SB();
        MM1(wC, 1, 0, 8, 5) LDW1(wB, 1, 10, 5) zz[13] = fmaf(zz[13], dnv[0], dnv[0]); PIN(zz[13]); tt[11] = tt[11] * zz[11]; PIN(tt[11]); SB();
        MM1(wC, 1, 1, 8, 5) tt[17] = fmaf(nm[1], csv[0][1], acc[1][0][1]); PIN(tt[17]); zz[15] = __builtin_amdgcn_exp2f(zz[15]); PIN(zz[15]); zz[16] = tt[16] * rzv[1]; PIN(zz[16]); loadA1(33); SB();
        MM1(wC, 2, 0, 8, 6) LDW1(wB, 2, 10, 6) zz[13] = __builtin_amdgcn_rcpf(zz[13]); PIN(zz[13]); zz[14] = fmaf(zz[14], dnv[0], dnv[0]); PIN(zz[14]); SB();
        MM1(wC, 2, 1, 8, 6) tt[12] = tt[12] * zz[12]; PIN(tt[12]); hst[0][2].y = pk2(tt[10], tt[11]); loadA1(34); SB();
        asm volatile("s_waitcnt lgkmcnt(3)" : "+v"(wA[0]), "+v"(wA[1]), "+v"(wA[2]) :: "memory");
        MM1(wA, 0, 0, 9, 4) LDW1(wC, 0, 11, 4) tt[18] = fmaf(nm[1], csv[0][2], acc[1][0][2]); PIN(tt[18]); zz[16] = __builtin_amdgcn_exp2f(zz[16]); PIN(zz[16]); SB();
        MM1(wA, 0, 1, 9, 4) zz[17] = tt[17] * rzv[1]; PIN(zz[17]); zz[14] = __builtin_amdgcn_rcpf(zz[14]); PIN(zz[14]);  SB();
        MM1(wA, 1, 0, 9, 5) LDW1(wC, 1, 11, 5) zz[15] = fmaf(zz[15], dnv[0], dnv[0]); PIN(zz[15]); tt[13] = tt[13] * zz[13]; PIN(tt[13]); SB();
        MM1(wA, 1, 1, 9, 5) tt[19] = fmaf(nm[1], csv[0][3], acc[1][0][3]); PIN(tt[19]); zz[17] = __builtin_amdgcn_exp2f(zz[17]); PIN(zz[17]); zz[18] = tt[18] * rzv[1]; PIN(zz[18]); loadA1(35); SB();
        MM1(wA, 2, 0, 9, 6) LDW1(wC, 2, 11, 6) zz[15] = __builtin_amdgcn_rcpf(zz[15]); PIN(zz[15]); zz[16] = fmaf(zz[16], dnv[1], dnv[1]); PIN(zz[16]); SB();
        MM1(wA, 2, 1, 9, 6) tt[14] = tt[14] * zz[14]; PIN(tt[14]); hst[0][3].x = pk2(tt[12], tt[13]); loadA1(36); SB();
        asm volatile("s_waitcnt lgkmcnt(3)" : "+v"(wB[0]), "+v"(wB[1]), "+v"(wB[2]) :: "memory");
        MM1(wB, 0, 0, 10, 4) LDW1(wA, 0, 12, 4) tt[20] = fmaf(nm[1], csv[1][0], acc[1][1][0]); PIN(tt[20]); zz[18] = __builtin_amdgcn_exp2f(zz[18]); PIN(zz[18]); SB();
        MM1(wB, 0, 1, 10, 4) zz[19] = tt[19] * rzv[1]; PIN(zz[19]); zz[16] = __builtin_amdgcn_rcpf(zz[16]); PIN(zz[16]);  SB();
        MM1(wB, 1, 0, 10, 5) LDW1(wA, 1, 12, 5) zz[17] = fmaf(zz[17], dnv[1], dnv[1]); PIN(zz[17]); tt[15] = tt[15] * zz[15]; PIN(tt[15]); SB();
        MM1(wB, 1, 1, 10, 5) tt[21] = fmaf(nm[1], csv[1][1], acc[1][1][1]); PIN(tt[21]); zz[19] = __builtin_amdgcn_exp2f(zz[19]); PIN(zz[19]); zz[20] = tt[20] * rzv[1]; PIN(zz[20]); loadA1(37); SB();
        MM1(wB, 2, 0, 10, 6) LDW1(wA, 2, 12, 6) zz[17] = __builtin_amdgcn_rcpf(zz[17]); PIN(zz[17]); zz[18] = fmaf(zz[18], dnv[1], dnv[1]); PIN(zz[18]); tt[16] = tt[16] * zz[16]; PIN(tt[16]); SB();
        MM1(wB, 2, 1, 10, 6) hst[0][3].y = pk2(tt[14], tt[15]); store_pair(0, 2); loadA1(38); SB();
        asm volatile("s_waitcnt lgkmcnt(3)" : "+v"(wC[0]), "+v"(wC[1]), "+v"(wC[2]) :: "memory");
        MM1(wC, 0, 0, 11, 4) LDW1(wB, 0, 13, 4) tt[22] = fmaf(nm[1], csv[1][2], acc[1][1][2]); PIN(tt[22]); zz[20] = __builtin_amdgcn_exp2f(zz[20]); PIN(zz[20]); SB();
        MM1(wC, 0, 1, 11, 4) zz[21] = tt[21] * rzv[1]; PIN(zz[21]); zz[18] = __builtin_amdgcn_rcpf(zz[18]); PIN(zz[18]);  SB();
        MM1(wC, 1, 0, 11, 5) LDW1(wB, 1, 13, 5) zz[19] = fmaf(zz[19], dnv[1], dnv[1]); PIN(zz[19]); tt[17] = tt[17] * zz[17]; PIN(tt[17]); SB();
        MM1(wC, 1, 1, 11, 5) tt[23] = fmaf(nm[1], csv[1][3], acc[1][1][3]); PIN(tt[23]); zz[21] = __builtin_amdgcn_exp2f(zz[21]); PIN(zz[21]); zz[22] = tt[22] * rzv[1]; PIN(zz[22]); loadA1(39); SB();
        MM1(wC, 2, 0, 11, 6) LDW1(wB, 2, 13, 6) zz[19] = __builtin_amdgcn_rcpf(zz[19]); PIN(zz[19]); zz[20] = fmaf(zz[20], dnv[1], dnv[1]); PIN(zz[20]); SB();
        MM1(wC, 2, 1, 11, 6) tt[18] = tt[18] * zz[18]; PIN(tt[18]); hst[1][0].x = pk2(tt[16], tt[17]); loadA1(40); SB();
        asm volatile("s_waitcnt lgkmcnt(3)" : "+v"(wA[0]), "+v"(wA[1]), "+v"(wA[2]) :: "memory");
        MM1(wA, 0, 0, 12, 4) LDW1(wC, 0, 14, 4) tt[24] = fmaf(nm[1], csv[2][0], acc[1][2][0]); PIN(tt[24]); zz[22] = __builtin_amdgcn_exp2f(zz[22]); PIN(zz[22]); SB();
        MM1(wA, 0, 1, 12, 4) zz[23] = tt[23] * rzv[1]; PIN(zz[23]); zz[20] = __builtin_amdgcn_rcpf(zz[20]); PIN(zz[20]); loadA1(41); SB();
        MM1(wA, 1, 0, 12, 5) LDW1(wC, 1, 14, 5) zz[21] = fmaf(zz[21], dnv[1], dnv[1]); PIN(zz[21]); tt[19] = tt[19] * zz[19]; PIN(tt[19]); SB();
        MM1(wA, 1, 1, 12, 5) tt[25] = fmaf(nm[1], csv[2][1], acc[1][2][1]); PIN(tt[25]); zz[23] = __builtin_amdgcn_exp2f(zz[23]); PIN(zz[23]); zz[24] = tt[24] * rzv[1]; PIN(zz[24]);  SB();
        MM1(wA, 2, 0, 12, 6) LDW1(wC, 2, 14, 6) zz[21] = __builtin_amdgcn_rcpf(zz[21]); PIN(zz[21]); zz[22] = fmaf(zz[22], dnv[1], dnv[1]); PIN(zz[22]); SB();
        MM1(wA, 2, 1, 12, 6) tt[20] = tt[20] * zz[20]; PIN(tt[20]); hst[1][0].y = pk2(tt[18], tt[19]); loadA1(42); SB();
        asm volatile("s_waitcnt lgkmcnt(3)" : "+v"(wB[0]), "+v"(wB[1]), "+v"(wB[2]) :: "memory");
        MM1(wB, 0, 0, 13, 4) LDW1(wA, 0, 15, 4) tt[26] = fmaf(nm[1], csv[2][2], acc[1][2][2]); PIN(tt[26]); zz[24] = __builtin_amdgcn_exp2f(zz[24]); PIN(zz[24]); SB();
        MM1(wB, 0, 1, 13, 4) zz[25] = tt[25] * rzv[1]; PIN(zz[25]); zz[22] = __builtin_amdgcn_rcpf(zz[22]); PIN(zz[22]); loadA1(43); SB();
        MM1(wB, 1, 0, 13, 5) LDW1(wA, 1, 15, 5) zz[23] = fmaf(zz[23], dnv[1], dnv[1]); PIN(zz[23]); tt[21] = tt[21] * zz[21]; PIN(tt[21]); SB();
        MM1(wB, 1, 1, 13, 5) tt[27] = fmaf(nm[1], csv[2][3], acc[1][2][3]); PIN(tt[27]); zz[25] = __builtin_amdgcn_exp2f(zz[25]); PIN(zz[25]); zz[26] = tt[26] * rzv[1]; PIN(zz[26]);  SB();
        MM1(wB, 2, 0, 13, 6) LDW1(wA, 2, 15, 6) zz[23] = __builtin_amdgcn_rcpf(zz[23]); PIN(zz[23]); zz[24] = fmaf(zz[24], dnv[1], dnv[1]); PIN(zz[24]); SB();
        MM1(wB, 2, 1, 13, 6) tt[22] = tt[22] * zz[22]; PIN(tt[22]); hst[1][1].x = pk2(tt[20], tt[21]); loadA1(44); SB();
        asm volatile("s_waitcnt lgkmcnt(3)" : "+v"(wC[0]), "+v"(wC[1]), "+v"(wC[2]) :: "memory");
        MM1(wC, 0, 0, 14, 4) LDW1(wB, 0, 16, 4) tt[28] = fmaf(nm[1], csv[3][0], acc[1][3][0]); PIN(tt[28]); zz[26] = __builtin_amdgcn_exp2f(zz[26]); PIN(zz[26]); SB();
        MM1(wC, 0, 1, 14, 4) zz[27] = tt[27] * rzv[1]; PIN(zz[27]); zz[24] = __builtin_amdgcn_rcpf(zz[24]); PIN(zz[24]); loadA1(45); SB();
        MM1(wC, 1, 0, 14, 5) LDW1(wB, 1, 16, 5) zz[25] = fmaf(zz[25], dnv[1], dnv[1]); PIN(zz[25]); tt[23] = tt[23] * zz[23]; PIN(tt[23]); SB();
        MM1(wC, 1, 1, 14, 5) tt[29] = fmaf(nm[1], csv[3][1], acc[1][3][1]); PIN(tt[29]); zz[27] = __builtin_amdgcn_exp2f(zz[27]); PIN(zz[27]); zz[28] = tt[28] * rzv[1]; PIN(zz[28]);  SB();
        MM1(wC, 2, 0, 14, 6) LDW1(wB, 2, 16, 6) zz[25] = __builtin_amdgcn_rcpf(zz[25]); PIN(zz[25]); zz[26] = fmaf(zz[26], dnv[1], dnv[1]); PIN(zz[26]); tt[24] = tt[24] * zz[24]; PIN(tt[24]); SB();
        MM1(wC, 2, 1, 14, 6) hst[1][1].y = pk2(tt[22], tt[23]); store_pair(1, 0); loadA1(46); SB();
        asm volatile("s_waitcnt lgkmcnt(3)" : "+v"(wA[0]), "+v"(wA[1]), "+v"(wA[2]) :: "memory");
        MM1(wA, 0, 0, 15, 4) LDW1(wC, 0, 17, 4) tt[30] = fmaf(nm[1], csv[3][2], acc[1][3][2]); PIN(tt[30]); zz[28] = __builtin_amdgcn_exp2f(zz[28]); PIN(zz[28]); SB();
        MM1(wA, 0, 1, 15, 4) zz[29] = tt[29] * rzv[1]; PIN(zz[29]); zz[26] = __builtin_amdgcn_rcpf(zz[26]); PIN(zz[26]); loadA1(47); SB();
        MM1(wA, 1, 0, 15, 5) LDW1(wC, 1, 17, 5) zz[27] = fmaf(zz[27], dnv[1], dnv[1]); PIN(zz[27]); tt[25] = tt[25] * zz[25]; PIN(tt[25]); SB();
        MM1(wA, 1, 1, 15, 5) tt[31] = fmaf(nm[1], csv[3][3], acc[1][3][3]); PIN(tt[31]); zz[29] = __builtin_amdgcn_exp2f(zz[29]); PIN(zz[29]); zz[30] = tt[30] * rzv[1]; PIN(zz[30]); loadA1(48); SB();
        MM1(wA, 2, 0, 15, 6) LDW1(wC, 2, 17, 6) zz[27] = __builtin_amdgcn_rcpf(zz[27]); PIN(zz[27]); zz[28] = fmaf(zz[28], dnv[1], dnv[1]); PIN(zz[28]); SB();
        MM1(wA, 2, 1, 15, 6) tt[26] = tt[26] * zz[26]; PIN(tt[26]); hst[1][2].x = pk2(tt[24], tt[25]);  SB();
        asm volatile("s_waitcnt lgkmcnt(3)" : "+v"(wB[0]), "+v"(wB[1]), "+v"(wB[2]) :: "memory");
        MM1(wB, 0, 0, 16, 4) LDW1(wA, 0, 18, 4) zz[30] = __builtin_amdgcn_exp2f(zz[30]); PIN(zz[30]); zz[31] = tt[31] * rzv[1]; PIN(zz[31]); SB();
        MM1(wB, 0, 1, 16, 4) zz[28] = __builtin_amdgcn_rcpf(zz[28]); PIN(zz[28]); zz[29] = fmaf(zz[29], dnv[1], dnv[1]); PIN(zz[29]); loadA1(49); SB();
        MM1(wB, 1, 0, 16, 5) LDW1(wA, 1, 18, 5) tt[27] = tt[27] * zz[27]; PIN(tt[27]); SB();
        MM1(wB, 1, 1, 16, 5) zz[31] = __builtin_amdgcn_exp2f(zz[31]); PIN(zz[31]); zz[29] = __builtin_amdgcn_rcpf(zz[29]); PIN(zz[29]); loadA1(50); SB();
        MM1(wB, 2, 0, 16, 6) LDW1(wA, 2, 18, 6) zz[30] = fmaf(zz[30], dnv[1], dnv[1]); PIN(zz[30]); tt[28] = tt[28] * zz[28]; PIN(tt[28]); SB();
        MM1(wB, 2, 1, 16, 6) hst[1][2].y = pk2(tt[26], tt[27]);  SB();
        asm volatile("s_waitcnt lgkmcnt(3)" : "+v"(wC[0]), "+v"(wC[1]), "+v"(wC[2]) :: "memory");
        MM1(wC, 0, 0, 17, 4) LDW1(wB, 0, 19, 4) zz[30] = __builtin_amdgcn_rcpf(zz[30]); PIN(zz[30]); SB();
        MM1(wC, 0, 1, 17, 4) zz[31] = fmaf(zz[31], dnv[1], dnv[1]); PIN(zz[31]); loadA1(51); SB();
        MM1(wC, 1, 0, 17, 5) LDW1(wB, 1, 19, 5) tt[29] = tt[29] * zz[29]; PIN(tt[29]); SB();
        MM1(wC, 1, 1, 17, 5) zz[31] = __builtin_amdgcn_rcpf(zz[31]); PIN(zz[31]); loadA1(52); SB();
        MM1(wC, 2, 0, 17, 6) LDW1(wB, 2, 19, 6) tt[30] = tt[30] * zz[30]; PIN(tt[30]); SB();
        MM1(wC, 2, 1, 17, 6) hst[1][3].x = pk2(tt[28], tt[29]); loadA1(53); SB();
        asm volatile("s_waitcnt lgkmcnt(3)" : "+v"(wA[0]), "+v"(wA[1]), "+v"(wA[2]) :: "memory");
        MM1(wA, 0, 0, 18, 4) tt[31] = tt[31] * zz[31]; PIN(tt[31]); SB();
        MM1(wA, 0, 1, 18, 4) SB();
        MM1(wA, 1, 0, 18, 5) SB();
        MM1(wA, 1, 1, 18, 5) hst[1][3].y = pk2(tt[30], tt[31]); SB();
        MM1(wA, 2, 0, 18, 6) store_pair(1, 2); SB();
        MM1(wA, 2, 1, 18, 6) SB();
        loadA1(54); loadA1(55); loadA1(56); SB();
        asm volatile("s_waitcnt lgkmcnt(0)" : "+v"(wB[0]), "+v"(wB[1]), "+v"(wB[2]) :: "memory");
        MM1(wB, 0, 0, 19, 4) SB();
        MM1(wB, 0, 1, 19, 4) loadA1(57); SB();
        MM1(wB, 1, 0, 19, 5) SB();
        MM1(wB, 1, 1, 19, 5) loadA1(58); SB();
        MM1(wB, 2, 0, 19, 6) SB();
        MM1(wB, 2, 1, 19, 6) loadA1(59); SB();
        STAMP(ws, 2, L * 8 + 3);
#pragma unroll
        for (int r = 0; r < 2; ++r) { epi(r, 4); epi(r, 5); epi(r, 6); }
        SB();
        asm volatile("s_waitcnt vmcnt(%0)" :: "n"(NA_IN(18, 20)) : "memory");
        publish_count(ws, L * 32 + rp);
#pragma unroll
        for (int r = 0; r < 2; ++r) {
            store_pair(r, 4);
            const int nb = nb_of(ct, 6);
            __builtin_amdgcn_raw_buffer_store_b64(hst[r][6], rs, voff + (nb & 1) * 8, hoff + ((rb0 + r) * KS2 + (nb >> 1)) * 1024, 16);
        }
        SB();
#pragma unroll
        for (int f = 60; f < 70; ++f) loadA1(f);
        SB();
        asm volatile("s_waitcnt vmcnt(%0)" :: "n"(F0 > 60 ? 70 - F0 : 10) : "memory");
    }
    publish_count(ws, L * 32 + 16 + rp);
    STAMP(ws, 2, L * 8 + 4);
    wait_count(ws, L * 32 + (rq >> 1), 16u);
    STAMP(ws, 2, L * 8 + 5);
    {
        int hoffc[4];
#pragma unroll
        for (int c = 0; c < 4; ++c) hoffc[c] = hoff + (rq * 4 + ((wv + c) & 3)) * (KS2 * 1024);
        u32x2 yv[5];
        if (L == 0) {
#pragma unroll
            for (int j = 0; j < 5; ++j) {
                const int db = db_of(cu, j);
                const int so = ((rq * 4 + wv) * KS1 + (db >> 1)) * 1024 + (db & 1) * 512;
                xres[j] = __builtin_amdgcn_raw_buffer_load_b64(rs, lane * 8, (int)OFF_X1T + so, 0);
                yv[j] = __builtin_amdgcn_raw_buffer_load_b64(rs, lane * 8, (int)OFF_YT + so, 0);
            }
            SB();
        }
        u32x4 B[G2_PH][4];
        auto loadB = [&](int st, int i) {
#pragma unroll
            for (int c = 0; c < 4; ++c) B[st][c] = __builtin_amdgcn_raw_buffer_load_b128(rs, voff, hoffc[c] + kst[i] * 1024, 16);
        };
#pragma unroll
        for (int p = 0; p < G2_PH - 1; ++p) { loadB(p, p); SB(); }
        f32x4 acc[5][4];
        unsigned early = 0;
#pragma unroll
        for (int i = 0; i < 14; ++i) {
            const int st = i % G2_PH;
            if (i + G2_PH - 1 == 5) {
                const char* cb = ws + OFF_CNT + (size_t)(L * 32 + 16 + (rq >> 1)) * 256;
                asm volatile("s_nop 4\n\tglobal_load_dword %0, %1, %2 sc1" : "=v"(early) : "v"(0u), "s"(cb) : "memory");
            }
            if (i + G2_PH - 1 == 8) {
                gu32* c = (gu32*)(ws + OFF_CNT + (size_t)(L * 32 + 16 + (rq >> 1)) * 256);
                unsigned spins = 0;
                asm volatile("s_waitcnt vmcnt(12)" : "+v"(early) :: "memory");
                if (__builtin_amdgcn_readfirstlane(early) < 16u)
                while (__builtin_amdgcn_readfirstlane(__hip_atomic_load(c, __ATOMIC_RELAXED, __HIP_MEMORY_SCOPE_AGENT)) < 16u) {
                    if (++spins > SPIN_CAP) break;
                }
                __builtin_amdgcn_fence(__ATOMIC_ACQUIRE, "wavefront");
                SB();
            }
            if (i + G2_PH - 1 < 14) loadB((i + G2_PH - 1) % G2_PH, i + G2_PH - 1);
            SB();
            if ((i != 7 || wv != 3) && (i != 13 || wv != 2)) {
#pragma unroll
                for (int c = 0; c < 4; ++c)
#pragma unroll
                    for (int j = 0; j < 5; ++j) {
 if (i == 0) asm volatile("v_mfma_f32_16x16x32_bf16 %0, %1, %2, 0" : "=&v"(acc[j][c]) : "a"(A[i][j]), "v"(B[st][c]));
                        else if (i * 5 + j < G2_NAG) asm volatile("v_mfma_f32_16x16x32_bf16 %0, %1, %2, %0" : "+v"(acc[j][c]) : "a"(A[i][j]), "v"(B[st][c]));
                        else                asm volatile("s_nop 1\n\tv_mfma_f32_16x16x32_bf16 %0, %1, %2, %0" : "+v"(acc[j][c]) : "v"(A[i][j]), "v"(B[st][c]));
                    }
            }
            SB();
        }
        asm volatile("s_nop 15\n\ts_nop 15" ::: "memory");
        SB();
        STAMP(ws, 2, L * 8 + 6);
        f32x4* red = (f32x4*)(smem + G1_LDS);
        f32x4 o[5];
#pragma unroll
        for (int j = 0; j < 5; ++j) o[j] = acc[j][0];
#pragma unroll
        for (int t = 1; t < 4; ++t) {
            const int dst = (wv + t) & 3;
#pragma unroll
            for (int j = 0; j < 5; ++j) red[(dst * 5 + j) * 64 + lane] = acc[j][t];
            __syncthreads();
#pragma unroll
            for (int j = 0; j < 5; ++j) o[j] += red[(wv * 5 + j) * 64 + lane];
            if (t < 3) __syncthreads();
        }
        const int r = lane & 15;
        const int rb = rq * 4 + wv;
        float x2[5][4];
#pragma unroll
        for (int j = 0; j < 5; ++j) {
            unsigned p01 = pk2(o[j][0], o[j][1]), p23 = pk2(o[j][2], o[j][3]);
            unsigned s01 = pk2(bflo(xres[j].x) + bflo(p01), bfhi(xres[j].x) + bfhi(p01));
            unsigned s23 = pk2(bflo(xres[j].y) + bflo(p23), bfhi(xres[j].y) + bfhi(p23));
            if (L == 1) {
                u32x2 st = {s01, s23};
                *(u32x2*)(out + ((size_t)(rb * 16 + r) * D_ + db_of(cu, j) * 16 + 4 * q) * 2) = st;
            } else {
                x2[j][0] = bflo(s01); x2[j][1] = bfhi(s01); x2[j][2] = bflo(s23); x2[j][3] = bfhi(s23);
            }
        }
        if (L == 0) {
            float s = 0.f, m2 = 0.f;
#pragma unroll
            for (int j = 0; j < 5; ++j)
#pragma unroll
                for (int e = 0; e < 4; ++e) { s += x2[j][e]; m2 = fmaf(x2[j][e], x2[j][e], m2); }
            s = rowsum4(s); m2 = rowsum4(m2);
            gu64* gr = (gu64*)(ws + OFF_GR) + (size_t)rb * 128 + r;
            if (q == 0) {
                unsigned us = __float_as_uint(s), um = __float_as_uint(m2);
                if (us == 0xffffffffu) us = 0x7fc00000u;
                if (um == 0xffffffffu) um = 0x7fc00000u;
                __hip_atomic_store(gr + cu * 16, ((unsigned long long)um << 32) | us, __ATOMIC_RELAXED, __HIP_MEMORY_SCOPE_AGENT);
            }
            float yf[5][4];
#pragma unroll
            for (int j = 0; j < 5; ++j) { yf[j][0] = bflo(yv[j].x); yf[j][1] = bfhi(yv[j].x); yf[j][2] = bflo(yv[j].y); yf[j][3] = bfhi(yv[j].y); }
            unsigned long long g0, g1;
            for (unsigned spins = 0;; ++spins) {
                g0 = __hip_atomic_load(gr + 32 * q, __ATOMIC_RELAXED, __HIP_MEMORY_SCOPE_AGENT);
                g1 = __hip_atomic_load(gr + 32 * q + 16, __ATOMIC_RELAXED, __HIP_MEMORY_SCOPE_AGENT);
                bool ok = ((unsigned)g0 != 0xffffffffu) && ((unsigned)(g0 >> 32) != 0xffffffffu) &&
                          ((unsigned)g1 != 0xffffffffu) && ((unsigned)(g1 >> 32) != 0xffffffffu);
                if (__all(ok) || spins > SPIN_CAP) break;
            }
            STAMP(ws, 2, 7);
            const float st = rowsum4(__uint_as_float((unsigned)g0) + __uint_as_float((unsigned)g1));
            const float vt = rowsum4(__uint_as_float((unsigned)(g0 >> 32)) + __uint_as_float((unsigned)(g1 >> 32)));
            const float mean = st * (1.f / D_);
            const float var = fmaxf(vt * (1.f / D_) - mean * mean, 0.f);
            const float m = rbf(mean);
            const float rinv = __builtin_amdgcn_rcpf(rbf(sqrtf(rbf(rbf(var) + 1e-5f))));
            const float nmr = -m * rinv;
            float s3 = 0.f, m23 = 0.f;
#pragma unroll
            for (int j = 0; j < 5; ++j) {
                float v4[4];
#pragma unroll
                for (int e = 0; e < 4; ++e) v4[e] = fmaf(fmaf(x2[j][e], rinv, nmr), yf[j][e], x2[j][e]);
                u32x2 x3 = {pk2(v4[0], v4[1]), pk2(v4[2], v4[3])};
                xres[j] = x3;
                if (j & 1) {
                    const int db = db_of(cu, j - 1);
                    u32x4 s4 = {xres[j - 1].x, xres[j - 1].y, x3.x, x3.y};
                    store16_sc1(s4, rs, voff, (int)OFF_X3T + (rb * KS1 + (db >> 1)) * 1024);
                } else if (j == 4) {
                    const int db = db_of(cu, 4);
                    __builtin_amdgcn_raw_buffer_store_b64(x3, rs, voff + (db & 1) * 8, (int)OFF_X3T + (rb * KS1 + (db >> 1)) * 1024, 16);
                }
                const float f0 = bflo(x3.x), f1 = bfhi(x3.x), f2 = bflo(x3.y), f3 = bfhi(x3.y);
                s3 += (f0 + f1) + (f2 + f3);
                m23 = fmaf(f0, f0, fmaf(f1, f1, fmaf(f2, f2, fmaf(f3, f3, m23))));
            }
            s3 = rowsum4(s3); m23 = rowsum4(m23);
            if (q == 0) {
                u32x2 pv = {__float_as_uint(s3), __float_as_uint(m23)};
                __builtin_amdgcn_raw_buffer_store_b64(pv, rs, r * 64 + cu * 8, (int)OFF_GR2 + rb * 1024, 16);
            }
            VMWAIT(0);
            publish_count(ws, 64 + (rq >> 1));
            wait_count(ws, 64 + rp, 16u);
            STAMP(ws, 2, 8);
        }
    }
}

__global__ __launch_bounds__(256, 1) void k2_ffn(char* __restrict__ ws, char* __restrict__ out) {
    extern __shared__ __attribute__((aligned(16))) char smem[];
    STAMP(ws, 2, 0);
    const int b = blockIdx.x;
    const int xcd = b & 7, idx = b >> 3;
    const int rp = 4 * (xcd >> 1) + (idx >> 3), ct = 8 * (xcd & 1) + (idx & 7);
    const int rq = 4 * xcd + (idx >> 3), cu = idx & 7;
    const int lane = threadIdx.x & 63;
    const int wv = __builtin_amdgcn_readfirstlane(threadIdx.x >> 6);
    const __amdgpu_buffer_rsrc_t rs = __builtin_amdgcn_make_buffer_rsrc((void*)ws, 0, (int)WS_USED, 0x00020000);
    u32x2 xres[5];
    SB();
    bf16x8 A[14][5];
    ffn_layer<0>(ws, out, smem, rs, rp, ct, rq, cu, lane, wv, xres, A);
    ffn_layer<1>(ws, out, smem, rs, rp, ct, rq, cu, lane, wv, xres, A);
}

extern "C" void kernel_launch(void* const* d_in, const int* in_sizes, int n_in,
                              void* d_out, int out_size, void* d_ws, size_t ws_size,
                              hipStream_t stream) {
    char* ws = (char*)d_ws;
    hipFuncSetAttribute((const void*)k1_conv, hipFuncAttributeMaxDynamicSharedMemorySize, K1_LDS);
    hipFuncSetAttribute((const void*)k2_ffn, hipFuncAttributeMaxDynamicSharedMemorySize, FFN_LDS);
    k0_prep<<<256, 896, 0, stream>>>((const unsigned*)d_in[0], (const unsigned*)d_in[1], (const unsigned*)d_in[2],
                                     (const u32x4*)d_in[3], (const u32x4*)d_in[4], (const u32x4*)d_in[7], (const u32x4*)d_in[8], ws);
    k1_conv<<<160 + 27 + 30, 512, K1_LDS, stream>>>((const unsigned short*)d_in[4], (const unsigned short*)d_in[5],
                                                    (const unsigned*)d_in[7], (const unsigned*)d_in[8], ws);
    k2_ffn<<<256, 256, FFN_LDS, stream>>>(ws, (char*)d_out);
}
```

```cpp
#include <hip/hip_runtime.h>
#include <hip/hip_bf16.h>
#include <stdint.h>

typedef __attribute__((ext_vector_type(8))) short bf16x8;
typedef __attribute__((ext_vector_type(4))) float f32x4;
typedef __attribute__((ext_vector_type(2))) float f32x2;
typedef __attribute__((ext_vector_type(4))) unsigned int u32x4;
typedef __attribute__((ext_vector_type(2))) unsigned int u32x2;
typedef __attribute__((ext_vector_type(2))) __bf16 bf2_t;

constexpr int S_ = 2048, D_ = 640, F_ = 1728;
constexpr int KS1 = D_ / 32;
constexpr int KS2 = F_ / 32;
constexpr int NB1 = F_ / 16;

constexpr size_t SZ_ACT = (size_t)S_ * D_ * 2;
constexpr size_t OFF_URT = 0;
constexpr size_t OFF_X0T = OFF_URT + SZ_ACT;
constexpr size_t OFF_Q0T = OFF_X0T + SZ_ACT;
constexpr size_t OFF_X1T = OFF_Q0T + SZ_ACT;
constexpr size_t OFF_YT  = OFF_X1T + SZ_ACT;
constexpr size_t OFF_X2T = OFF_YT + SZ_ACT;
constexpr size_t OFF_X3T = OFF_X2T + SZ_ACT;
constexpr size_t OFF_HT  = OFF_X3T + SZ_ACT;
constexpr size_t SZ_HT   = (size_t)S_ * F_ * 2;
constexpr size_t OFF_W1T = OFF_HT + SZ_HT;
constexpr size_t SZ_W    = (size_t)D_ * F_ * 2;
constexpr size_t OFF_W2T = OFF_W1T + SZ_W;
constexpr size_t OFF_CS  = OFF_W2T + SZ_W;
constexpr size_t OFF_HT2 = OFF_CS + 8192;
constexpr size_t OFF_GR  = OFF_HT2 + SZ_HT;
constexpr size_t OFF_GR2 = OFF_GR + (size_t)S_ * 8 * 8;
constexpr size_t OFF_CNT = OFF_GR2 + (size_t)S_ * 8 * 8;
constexpr size_t WS_USED = OFF_CNT + 5 * 16 * 256;

#define STAMP_DECL
#define STAMP(ws_, kid, i) do {} while (0)
#define STAMP_FLUSH(ws_, kid) do {} while (0)
#define STAMP_END(ws_, kid, i) do {} while (0)
#define SB() __builtin_amdgcn_sched_barrier(0)

__device__ __forceinline__ float bflo(unsigned x) { return __uint_as_float(x << 16); }
__device__ __forceinline__ float bfhi(unsigned x) { return __uint_as_float(x & 0xffff0000u); }
__device__ __forceinline__ unsigned pk2(float lo, float hi) {
    f32x2 v = {lo, hi};
    bf2_t r = __builtin_convertvector(v, bf2_t);
    return __builtin_bit_cast(unsigned, r);
}
__device__ __forceinline__ float rbf(float x) { return bflo(pk2(x, 0.f)); }

__device__ __forceinline__ float rowsum4(float x) {
    unsigned u = __float_as_uint(x);
    auto a = __builtin_amdgcn_permlane16_swap(u, u, false, false);
    float y = __uint_as_float(a[0]) + __uint_as_float(a[1]);
    unsigned v = __float_as_uint(y);
    auto c = __builtin_amdgcn_permlane32_swap(v, v, false, false);
    return __uint_as_float(c[0]) + __uint_as_float(c[1]);
}
__device__ __forceinline__ float halfsum32(float x) {
    x += __builtin_bit_cast(float, __builtin_amdgcn_update_dpp(0, __builtin_bit_cast(int, x), 0xB1, 0xF, 0xF, true));
    x += __builtin_bit_cast(float, __builtin_amdgcn_update_dpp(0, __builtin_bit_cast(int, x), 0x4E, 0xF, 0xF, true));
    x += __builtin_bit_cast(float, __builtin_amdgcn_update_dpp(0, __builtin_bit_cast(int, x), 0x141, 0xF, 0xF, true));
    x += __builtin_bit_cast(float, __builtin_amdgcn_update_dpp(0, __builtin_bit_cast(int, x), 0x140, 0xF, 0xF, true));
    unsigned u = __float_as_uint(x);
    auto a = __builtin_amdgcn_permlane16_swap(u, u, false, false);
    return __uint_as_float(a[0]) + __uint_as_float(a[1]);
}
__device__ __forceinline__ void gst16_wt(void* p, u32x4 v) {
    asm volatile("global_store_dwordx4 %0, %1, off sc1\n\ts_nop 1" :: "v"(p), "v"(v) : "memory");
}

__global__ __launch_bounds__(896) void k0_prep(const unsigned* __restrict__ q, const unsigned* __restrict__ k,
                                               const unsigned* __restrict__ v, const u32x4* __restrict__ shk,
                                               const u32x4* __restrict__ ssmk, const u32x4* __restrict__ w1,
                                               const u32x4* __restrict__ w2, char* __restrict__ ws) {
    __shared__ __attribute__((aligned(16))) char xs[160 * 8 * 16];
    const int t = threadIdx.x;
    const int b = (blockIdx.x & 7) * 32 + (blockIdx.x >> 3);
    const int s0 = b * 8;
    STAMP_DECL;
    STAMP(ws, 0, 0);
    if (t < 640) {
        const int cp = t % 320, rh = t / 320;
        const int sb = s0 + 4 * rh;
        unsigned kr[7], vr[4];
#pragma unroll
        for (int i = 0; i < 7; ++i) {
            int s = sb - 3 + i;
            kr[i] = k[(size_t)(s > 0 ? s : 0) * (D_ / 2) + cp];
        }
#pragma unroll
        for (int i = 0; i < 4; ++i) vr[i] = v[(size_t)(sb + i) * (D_ / 2) + cp];
        const u32x4 w = shk[cp];
        unsigned long long pc_;
        asm volatile("s_getpc_b64 %0" : "=s"(pc_));
        const char* tx_ = (const char*)(pc_ & ~0xffull);
        u32x4 pf0 = *(const u32x4*)(tx_ + (size_t)((blockIdx.x >> 3) * 92 + (t < 92 ? t : 0)) * 16);
        u32x4 pf1 = pf0, pf2 = pf0;
        SB();
#pragma unroll
        for (int i = 0; i < 3; ++i) kr[i] = (sb - 3 + i >= 0) ? kr[i] : 0u;
        const float wa[4] = {bflo(w.x), bfhi(w.x), bflo(w.y), bfhi(w.y)};
        const float wb[4] = {bflo(w.z), bfhi(w.z), bflo(w.w), bfhi(w.w)};
        float ua[4], ub[4];
#pragma unroll
        for (int i = 0; i < 4; ++i) {
            unsigned acc = pk2(bflo(kr[i]) * wa[3], bfhi(kr[i]) * wb[3]);
#pragma unroll
            for (int j = 1; j < 4; ++j) {
                unsigned pr = pk2(bflo(kr[i + j]) * wa[3 - j], bfhi(kr[i + j]) * wb[3 - j]);
                acc = pk2(bflo(acc) + bflo(pr), bfhi(acc) + bfhi(pr));
            }
            ua[i] = bflo(acc) * bflo(vr[i]);
            ub[i] = bfhi(acc) * bfhi(vr[i]);
        }
        u32x2 oa = {pk2(ua[3], ua[2]), pk2(ua[1], ua[0])};
        u32x2 ob = {pk2(ub[3], ub[2]), pk2(ub[1], ub[0])};
        const int d = 2 * cp;
        char* dst = ws + OFF_URT + ((((size_t)(d >> 2) * 256 + (255 - b)) * 4 + (d & 3)) * 16) + (1 - rh) * 8;
        *(u32x2*)dst = oa;
        *(u32x2*)(dst + 16) = ob;
        asm volatile("" :: "v"(pf0), "v"(pf1), "v"(pf2));
    } else {
        const int tt = t - 640;
        if (tt < 64) ((u32x2*)(ws + OFF_GR))[(size_t)s0 * 8 + tt] = (u32x2){0xffffffffu, 0xffffffffu};
        if (b == 0 && tt >= 64 && tt < 64 + 80) ((unsigned*)(ws + OFF_CNT))[(tt - 64) * 64] = 0u;
        const int row = tt >> 5, ll = tt & 31;
        const int s = s0 + row;
        u32x2 xq[5];
#pragma unroll
        for (int i = 0; i < 5; ++i) xq[i] = *(const u32x2*)(q + (size_t)s * (D_ / 2) + 2 * (ll + 32 * i));
        float xv[5][4];
        float sum = 0.f;
#pragma unroll
        for (int i = 0; i < 5; ++i) {
            xv[i][0] = bflo(xq[i].x); xv[i][1] = bfhi(xq[i].x); xv[i][2] = bflo(xq[i].y); xv[i][3] = bfhi(xq[i].y);
            sum += (xv[i][0] + xv[i][1]) + (xv[i][2] + xv[i][3]);
        }
        float sq = 0.f;
#pragma unroll
        for (int i = 0; i < 5; ++i)
#pragma unroll
            for (int e = 0; e < 4; ++e) sq = fmaf(xv[i][e], xv[i][e], sq);
        sum = halfsum32(sum); sq = halfsum32(sq);
        const float mean = sum * (1.f / D_);
        const float var = fmaxf(sq * (1.f / D_) - mean * mean, 0.f);
        const float m = rbf(mean);
        const float rinv = __builtin_amdgcn_rcpf(rbf(sqrtf(rbf(rbf(var) + 1e-5f))));
#pragma unroll
        for (int i = 0; i < 5; ++i) {
            const int g = ll + 32 * i;
            unsigned c01 = pk2(xv[i][0] - m, xv[i][1] - m), c23 = pk2(xv[i][2] - m, xv[i][3] - m);
            u32x2 o = {pk2(bflo(c01) * rinv, bfhi(c01) * rinv), pk2(bflo(c23) * rinv, bfhi(c23) * rinv)};
            *(u32x4*)(xs + (g * 8 + row) * 16) = (u32x4){xq[i].x, xq[i].y, o.x, o.y};
        }
    }
    __syncthreads();
    if (t < 640) {
#pragma unroll
        for (int e = 0; e < 2; ++e) {
            const int c = t + 640 * e;
            gst16_wt(ws + OFF_X0T + ((size_t)(c >> 3) * S_ + s0 + (c & 7)) * 16, *(const u32x4*)(xs + c * 16));
        }
    }
    STAMP(ws, 0, 1);
    STAMP_END(ws, 0, 2);
    STAMP_FLUSH(ws, 0);
}

constexpr int HC_STRIDE = 4160;
constexpr int CH_H = 4 * HC_STRIDE;
constexpr int UR_BYTES = 4608;
constexpr int CH_BYTES = CH_H + UR_BYTES;
constexpr int K1_RED_OFF = 4 * CH_BYTES;
constexpr int K1_LDS = K1_RED_OFF + 8 * 8192;

__device__ __forceinline__ void retile_block(const unsigned* __restrict__ W, int ncols, int nks, int cg, int u0, int u1,
                                             char* __restrict__ dstT, float* __restrict__ cs, float* lds) {
    const int t = threadIdx.x, lane = t & 63, wv = t >> 6;
    const int cp = lane & 31, hh = lane >> 5;
    const int n0 = cg * 64 + 2 * cp;
    float sa = 0.f, sb = 0.f;
    unsigned val[5][8];
#pragma unroll
    for (int i = 0; i < 5; ++i) {
        int u = u0 + wv * 10 + 2 * i + hh;
        int uu = u < u1 ? u : u1 - 1;
        int ks = uu >> 2, q = uu & 3;
#pragma unroll
        for (int j = 0; j < 8; ++j) {
            int kk = 32 * ks + (j >> 2) * 16 + 4 * q + (j & 3);
            val[i][j] = W[(size_t)kk * (ncols / 2) + (n0 >> 1)];
        }
    }
#pragma unroll
    for (int i = 0; i < 5; ++i) {
        int u = u0 + wv * 10 + 2 * i + hh;
        if (u < u1) {
            u32x4 lo, hi;
            lo.x = __builtin_amdgcn_perm(val[i][1], val[i][0], 0x05040100u);
            lo.y = __builtin_amdgcn_perm(val[i][3], val[i][2], 0x05040100u);
            lo.z = __builtin_amdgcn_perm(val[i][5], val[i][4], 0x05040100u);
            lo.w = __builtin_amdgcn_perm(val[i][7], val[i][6], 0x05040100u);
            hi.x = __builtin_amdgcn_perm(val[i][1], val[i][0], 0x07060302u);
            hi.y = __builtin_amdgcn_perm(val[i][3], val[i][2], 0x07060302u);
            hi.z = __builtin_amdgcn_perm(val[i][5], val[i][4], 0x07060302u);
            hi.w = __builtin_amdgcn_perm(val[i][7], val[i][6], 0x07060302u);
            u32x4* stg = (u32x4*)(lds + 2048) + (wv * 5 + i) * 128;
            stg[hh * 64 + 2 * cp] = lo;
            stg[hh * 64 + 2 * cp + 1] = hi;
#pragma unroll
            for (int j = 0; j < 8; ++j) { sa += bflo(val[i][j]); sb += bfhi(val[i][j]); }
        }
    }
    asm volatile("s_waitcnt lgkmcnt(0)" ::: "memory");
#pragma unroll
    for (int i = 0; i < 5; ++i)
#pragma unroll
        for (int e = 0; e < 2; ++e) {
            const int u = u0 + wv * 10 + 2 * i + e;
            if (u < u1) {
                const u32x4 vv = ((const u32x4*)(lds + 2048))[(wv * 5 + i) * 128 + e * 64 + lane];
                const int n = cg * 64 + lane;
                gst16_wt(dstT + (((size_t)(n >> 4) * nks + (u >> 2)) * 64 + (u & 3) * 16 + (n & 15)) * 16, vv);
            }
        }
    if (cs) {
        lds[(wv * 2 + hh) * 64 + 2 * cp] = sa;
        lds[(wv * 2 + hh) * 64 + 2 * cp + 1] = sb;
        __syncthreads();
        if (t < 64) {
            float s = 0.f;
#pragma unroll
            for (int i = 0; i < 16; ++i) s += lds[i * 64 + t];
            cs[cg * 64 + t] = s;
        }
    }
}

__global__ __launch_bounds__(512) void k1_conv(const unsigned short* __restrict__ ssmk, const unsigned short* __restrict__ ssmD,
                                               const unsigned* __restrict__ w1, const unsigned* __restrict__ w2,
                                               char* __restrict__ ws) {
    extern __shared__ __attribute__((aligned(16))) char smem[];
    const int b = blockIdx.x, t = threadIdx.x;
    if (b >= 160) {
        STAMP_DECL;
        STAMP(ws, 7, 0);
        int r = b - 160;
        if (r < 27) {
            retile_block(w1, F_, KS1, r, 0, 80, ws + OFF_W1T, (float*)(ws + OFF_CS), (float*)smem);
        } else {
            r -= 27;
            int cg = r / 3, part = r % 3;
            int u0 = part * 80, u1 = (u0 + 80 < 216) ? u0 + 80 : 216;
            retile_block(w2, D_, KS2, cg, u0, u1, ws + OFF_W2T, nullptr, (float*)smem);
        }
        STAMP_END(ws, 7, 1);
        STAMP_FLUSH(ws, 7);
        return;
    }
    STAMP_DECL;
    STAMP(ws, 1, 0);
    const int pi_ = (b & 7) * 10 + (b >> 4);
    const int gch = (pi_ >> 2) * 8 + ((b >> 3) & 1) * 4 + (pi_ & 3);
    const int d0 = 4 * gch;
    const int lane = t & 63, wv = __builtin_amdgcn_readfirstlane(t >> 6);
    {
        const u32x4* src = (const u32x4*)(ws + OFF_URT) + (size_t)gch * 1024;
        const int hch = t >> 7, hi = t & 127;
        const unsigned short* hp = ssmk + (size_t)(d0 + hch) * S_;
        u32x4 ha[2]; u32x2 hb[2];
#pragma unroll
        for (int e = 0; e < 2; ++e) {
            const int m = hi + 128 * e;
            ha[e] = *(const u32x4*)(hp + 8 * m);
            hb[e] = *(const u32x2*)(hp + (m < 255 ? 8 * m + 8 : 0));
        }
        u32x4 a0 = src[t], a1 = src[t + 512];
        SB();
        char* cbase = smem + hch * CH_BYTES;
        if (hi == 0) {
            u32x4 z = {0u, 0u, 0u, 0u};
            u32x4 c1 = z, c2 = z, c3 = z;
            c1.w = ha[0].x << 16;
            c2.w = ha[0].x;
            c3.z = ha[0].x << 16; c3.w = __builtin_amdgcn_alignbit(ha[0].y, ha[0].x, 16);
            *(u32x4*)(cbase + 16) = z;
            *(u32x4*)(cbase + 16 + HC_STRIDE) = c1;
            *(u32x4*)(cbase + 16 + 2 * HC_STRIDE) = c2;
            *(u32x4*)(cbase + 16 + 3 * HC_STRIDE) = c3;
        } else if (hi < 5) {
            *(u32x4*)(cbase + (hi - 1) * HC_STRIDE) = (u32x4){0u, 0u, 0u, 0u};
        } else if (hi < 37) {
            *(u32x4*)(cbase + CH_H + 4096 + (hi - 5) * 16) = (u32x4){0u, 0u, 0u, 0u};
        }
#pragma unroll
        for (int e = 0; e < 2; ++e) {
            char* base = cbase + (hi + 128 * e + 2) * 16;
            u32x4 A = ha[e]; u32x2 B = hb[e];
            u32x4 c1 = {__builtin_amdgcn_alignbit(A.y, A.x, 16), __builtin_amdgcn_alignbit(A.z, A.y, 16),
                        __builtin_amdgcn_alignbit(A.w, A.z, 16), __builtin_amdgcn_alignbit(B.x, A.w, 16)};
            u32x4 c2 = {A.y, A.z, A.w, B.x};
            u32x4 c3 = {c1.y, c1.z, c1.w, __builtin_amdgcn_alignbit(B.y, B.x, 16)};
            *(u32x4*)(base) = A;
            *(u32x4*)(base + HC_STRIDE) = c1;
            *(u32x4*)(base + 2 * HC_STRIDE) = c2;
            *(u32x4*)(base + 3 * HC_STRIDE) = c3;
        }
        *(u32x4*)(smem + (t & 3) * CH_BYTES + CH_H + (t >> 2) * 16) = a0;
        *(u32x4*)(smem + (t & 3) * CH_BYTES + CH_H + ((t + 512) >> 2) * 16) = a1;
    }
    const int eks = d0 >> 5, ehalf = (d0 >> 4) & 1, eq = (d0 >> 2) & 3;
    u32x4 xqr[4];
#pragma unroll
    for (int i = 0; i < 4; ++i) xqr[i] = *(const u32x4*)(ws + OFF_X0T + ((size_t)gch * S_ + t + 512 * i) * 16);
    const u32x2 dsk = *(const u32x2*)(ssmD + d0);
    SB();
    __syncthreads();
    STAMP(ws, 1, 1);
    {
        const int ch = wv & 3, bh = wv >> 2;
        const int jr = lane & 15, q = lane >> 4;
        const unsigned sbase = (unsigned)(size_t)(__attribute__((address_space(3))) char*)smem;
        const unsigned abase = sbase + ch * CH_BYTES + ((jr + 1) & 3) * HC_STRIDE + 16 * q + 2 * ((jr + 1) & ~3) + 64 * (4 * bh);
        const unsigned bbase = sbase + ch * CH_BYTES + CH_H + 2 * (2032 - 16 * jr + 8 * q) + 64 * (4 * bh) - 512 * 7;
        f32x4 acc[8];
#pragma unroll
        for (int c = 0; c < 8; ++c) acc[c] = (f32x4){0.f, 0.f, 0.f, 0.f};
        u32x2 Al[2][8], Ah[2][8];
        bf16x8 Bf[2][8];
#define K1_RA(st, a_, bb_)                                                                                      \
        asm volatile("ds_read_b64 %0, %1 offset:%2" : "=v"(Al[st][a_]) : "v"(abase), "i"(64 * (bb_) + 512 * (a_)) : "memory"); \
        asm volatile("ds_read_b64 %0, %1 offset:%2" : "=v"(Ah[st][a_]) : "v"(abase), "i"(64 * (bb_) + 512 * (a_) + 8) : "memory");
#define K1_RB(st, m_, bb_)                                                                                      \
        asm volatile("ds_read_b128 %0, %1 offset:%2" : "=v"(Bf[st][m_]) : "v"(bbase), "i"(64 * (bb_) + 512 * (7 - (m_))) : "memory");
#define K1_WAIT(st)                                                                                             \
        asm volatile("s_waitcnt lgkmcnt(0)"                                                                     \
                     : "+v"(Al[st][0]), "+v"(Al[st][1]), "+v"(Al[st][2]), "+v"(Al[st][3]), "+v"(Al[st][4]), "+v"(Al[st][5]), "+v"(Al[st][6]), "+v"(Al[st][7]), \
                       "+v"(Ah[st][0]), "+v"(Ah[st][1]), "+v"(Ah[st][2]), "+v"(Ah[st][3]), "+v"(Ah[st][4]), "+v"(Ah[st][5]), "+v"(Ah[st][6]), "+v"(Ah[st][7]) \
                     :: "memory");                                                                              \
        asm volatile(""                                                                                         \
                     : "+v"(Bf[st][0]), "+v"(Bf[st][1]), "+v"(Bf[st][2]), "+v"(Bf[st][3]), "+v"(Bf[st][4]), "+v"(Bf[st][5]), "+v"(Bf[st][6]), "+v"(Bf[st][7]) \
                     :: "memory");
#define K1_MROW(st, a_)                                                                                         \
        {                                                                                                       \
            u32x4 x_ = {Al[st][a_].x, Al[st][a_].y, Ah[st][a_].x, Ah[st][a_].y};                                \
            bf16x8 af_ = __builtin_bit_cast(bf16x8, x_);                                                        \
            _Pragma("unroll") for (int m = 0; m < 8 - (a_); ++m)                                                \
                acc[(a_) + m] = __builtin_amdgcn_mfma_f32_16x16x32_bf16(af_, Bf[st][m], acc[(a_) + m], 0, 0, 0); \
            SB();                                                                                               \
        }
#define K1_ITER(st, bb_, NEXT)                                                                                  \
        K1_WAIT(st)                                                                                             \
        K1_MROW(st, 0) if (NEXT) { K1_RB((st) ^ 1, 0, (bb_) + 1) K1_RB((st) ^ 1, 1, (bb_) + 1) K1_RB((st) ^ 1, 2, (bb_) + 1) K1_RB((st) ^ 1, 3, (bb_) + 1) K1_RA((st) ^ 1, 0, (bb_) + 1) } SB(); \
        K1_MROW(st, 1) if (NEXT) { K1_RB((st) ^ 1, 4, (bb_) + 1) K1_RB((st) ^ 1, 5, (bb_) + 1) K1_RB((st) ^ 1, 6, (bb_) + 1) K1_RB((st) ^ 1, 7, (bb_) + 1) K1_RA((st) ^ 1, 1, (bb_) + 1) } SB(); \
        K1_MROW(st, 2) if (NEXT) { K1_RA((st) ^ 1, 2, (bb_) + 1) K1_RA((st) ^ 1, 3, (bb_) + 1) } SB();          \
        K1_MROW(st, 3) if (NEXT) { K1_RA((st) ^ 1, 4, (bb_) + 1) K1_RA((st) ^ 1, 5, (bb_) + 1) } SB();          \
        K1_MROW(st, 4) if (NEXT) { K1_RA((st) ^ 1, 6, (bb_) + 1) K1_RA((st) ^ 1, 7, (bb_) + 1) } SB();          \
        K1_MROW(st, 5) K1_MROW(st, 6) K1_MROW(st, 7)
        K1_RB(0, 0, 0) K1_RA(0, 0, 0) K1_RB(0, 1, 0) K1_RB(0, 2, 0) K1_RB(0, 3, 0) K1_RB(0, 4, 0) K1_RB(0, 5, 0) K1_RB(0, 6, 0) K1_RB(0, 7, 0)
        K1_RA(0, 1, 0) K1_RA(0, 2, 0) K1_RA(0, 3, 0) K1_RA(0, 4, 0) K1_RA(0, 5, 0) K1_RA(0, 6, 0) K1_RA(0, 7, 0)
        K1_ITER(0, 0, true)
        K1_ITER(1, 1, true)
        K1_ITER(0, 2, true)
        K1_ITER(1, 3, false)
        float* red = (float*)(smem + K1_RED_OFF) + (ch * 2 + bh) * 2048;
#pragma unroll
        for (int c = 0; c < 8; ++c) *(f32x4*)(red + 256 * c + 16 * jr + 4 * (q ^ ((jr >> 1) & 3))) = acc[c];
    }
    __syncthreads();
    STAMP(ws, 1, 2);
    {
        const float* red = (const float*)(smem + K1_RED_OFF);
        float dk[4] = {bflo(dsk.x), bfhi(dsk.x), bflo(dsk.y), bfhi(dsk.y)};
#pragma unroll
        for (int i = 0; i < 4; ++i) {
            int s = t + 512 * i;
            const int sr = s ^ (((s >> 5) & 3) << 2);
            float xs[4] = {bflo(xqr[i].x), bfhi(xqr[i].x), bflo(xqr[i].y), bfhi(xqr[i].y)};
            float qs[4] = {bflo(xqr[i].z), bfhi(xqr[i].z), bflo(xqr[i].w), bfhi(xqr[i].w)};
            float yo[4], xo[4];
#pragma unroll
            for (int c = 0; c < 4; ++c) {
                float y = rbf(red[(c * 2) * 2048 + sr] + red[(c * 2 + 1) * 2048 + sr]);
                unsigned short uu = *(const unsigned short*)(smem + c * CH_BYTES + CH_H + 2 * (2047 - s));
                float u = bflo(uu);
                y = rbf(y + rbf(u * dk[c]));
                yo[c] = y;
                xo[c] = xs[c] + rbf(qs[c] * y);
            }
            size_t off = (((size_t)(s >> 4) * KS1 + eks) * 2 + ehalf) * 512 + (eq * 16 + (s & 15)) * 8;
            u32x2 oy = {pk2(yo[0], yo[1]), pk2(yo[2], yo[3])};
            u32x2 ox = {pk2(xo[0], xo[1]), pk2(xo[2], xo[3])};
            *(u32x2*)(ws + OFF_YT + off) = oy;
            *(u32x2*)(ws + OFF_X1T + (((size_t)(s >> 4) * KS1 + eks) * 64 + eq * 16 + (s & 15)) * 16 + ehalf * 8) = ox;
        }
    }
    STAMP(ws, 1, 3);
    STAMP_END(ws, 1, 4);
    STAMP_FLUSH(ws, 1);
}

constexpr int G1_LDS = KS1 * 7 * 1024;
constexpr int FFN_LDS = G1_LDS + 20480;
constexpr unsigned SPIN_CAP = 1u << 22;
#define SPIN_SLEEP() do {} while (0)
typedef __attribute__((address_space(1))) unsigned gu32;
typedef __attribute__((address_space(1))) unsigned long long gu64;

#define GLOAD_X4(dst, voff, sbase, imm) \
    asm volatile("global_load_dwordx4 %0, %1, %2 offset:%3 sc1" : "=v"(dst) : "v"(voff), "s"(sbase), "i"(imm) : "memory")
#define DSR(dst, addr, off) asm volatile("ds_read_b128 %0, %1 offset:%2" : "=v"(dst) : "v"(addr), "i"(off) : "memory")
__device__ __forceinline__ void store16_sc1(u32x4 v, const __amdgpu_buffer_rsrc_t rs, unsigned voff, int soff) {
    __builtin_amdgcn_raw_buffer_store_b128(v, rs, voff, soff, 16);
    asm volatile("s_nop 1" :: "v"(v) : "memory");
}
#define VMWAIT(n) asm volatile("s_waitcnt vmcnt(" #n ")" ::: "memory")

__device__ __forceinline__ void wait_count(char* ws, int idx, unsigned want) {
    if (threadIdx.x == 0) {
        gu32* c = (gu32*)(ws + OFF_CNT + (size_t)idx * 256);
        unsigned spins = 0;
        while (__hip_atomic_load(c, __ATOMIC_RELAXED, __HIP_MEMORY_SCOPE_AGENT) < want) {
            SPIN_SLEEP();
            if (++spins > SPIN_CAP) break;
        }
        __builtin_amdgcn_fence(__ATOMIC_ACQUIRE, "wavefront");
    }
    __builtin_amdgcn_s_barrier();
    asm volatile("" ::: "memory");
}
__device__ __forceinline__ void publish_count(char* ws, int idx) {
    __builtin_amdgcn_s_barrier();
    if (threadIdx.x == 0)
        __hip_atomic_fetch_add((gu32*)(ws + OFF_CNT + (size_t)idx * 256), 1u, __ATOMIC_RELAXED, __HIP_MEMORY_SCOPE_AGENT);
}

constexpr int G2_PH = 4;
constexpr int G2_NAG = 52;
constexpr int G2_KEEP = 9;
__device__ __forceinline__ int nb_of(int ct, int i) { int j = i + (ct & 1); j = j >= 7 ? j - 7 : j; return 7 * ct + j; }
__device__ __forceinline__ int db_of(int cu, int j) { int t = j + (cu & 1); t = t >= 5 ? t - 5 : t; return 5 * cu + t; }

__device__ __forceinline__ int ksA_of(int a) { return 7 * (a >> 2) + (a & 1) + ((a & 2) << 1); }
__device__ __forceinline__ int ksB_of(int b) { int g = b / 3, r = b - 3 * g; return 7 * g + (r == 2 ? 6 : r + 2); }

template <int L>
__device__ __forceinline__ void ffn_layer(char* __restrict__ ws, char* __restrict__ out, char* smem, const __amdgpu_buffer_rsrc_t rs,
                                          const int rp, const int ct, const int rq, const int cu, const int lane, const int wv,
                                          u32x2 (&xres)[5], bf16x8 (&A)[14][5]
                                          ) {
    const int rb0 = rp * 8 + 2 * wv;
    const int q = lane >> 4;
    const unsigned voff = lane * 16;
    int kst[14];
#pragma unroll
    for (int i = 0; i < 8; ++i) { int a = 8 * wv + i; kst[i] = ksA_of(a < 31 ? a : 30); }
#pragma unroll
    for (int i = 0; i < 6; ++i) { int bb = (wv < 3 ? 6 * wv : 17) + i; if (wv == 2 && i == 5) bb = 16; kst[8 + i] = ksB_of(bb); }
    int wsoff[5];
#pragma unroll
    for (int j = 0; j < 5; ++j) wsoff[j] = db_of(cu, j) * KS2 * 1024;
    const char* w2base = ws + OFF_W2T;
    unsigned voffA[5];
#pragma unroll
    for (int j = 0; j < 5; ++j) voffA[j] = voff + wsoff[j];
    auto loadA1 = [&](int f) {
        const int g = f / 5, j = f % 5;
        if (f < 70 && (L == 0 || g >= G2_KEEP)) {
            const char* pb = w2base + kst[g] * 1024;
            if (f < G2_NAG) asm volatile("global_load_dwordx4 %0, %1, %2" : "=a"(A[g][j]) : "v"(voffA[j]), "s"(pb) : "memory");
            else A[g][j] = __builtin_bit_cast(bf16x8, __builtin_amdgcn_raw_buffer_load_b128(rs, voff, (int)OFF_W2T + wsoff[j] + kst[g] * 1024, 0));
        }
    };
    constexpr int F0 = (L == 0) ? 0 : 5 * G2_KEEP;
    const int hoff = (int)(L == 0 ? OFF_HT : OFF_HT2);
    {
        const char* xbase = ws + (L == 0 ? OFF_X1T : OFF_X3T) + (size_t)rb0 * (KS1 * 1024);
        const char* W1T = ws + OFF_W1T;
        f32x4 csv[7];
#pragma unroll
        for (int i = 0; i < 7; ++i) {
            int nb = nb_of(ct, i); nb = nb < NB1 ? nb : NB1 - 1;
            const char* cb = ws + OFF_CS + nb * 64;
            asm volatile("s_nop 4\n\tglobal_load_dwordx4 %0, %1, %2" : "=v"(csv[i]) : "v"(q * 16), "s"(cb) : "memory");
        }
        SB();
        bf16x8 xf[2][KS1];
        u32x4 pst[2];
        const unsigned voffB = voff + KS1 * 1024;
        const unsigned lds0 = (unsigned)(size_t)(__attribute__((address_space(3))) char*)smem;
#define LX(r, ks) { unsigned v_ = ((r) ? voffB : voff) + ((ks) >> 2) * 4096; GLOAD_X4(xf[r][ks], v_, xbase, ((ks) & 3) * 1024); }
#define LW(c, i)                                                                                                \
    {                                                                                                           \
        int nb = nb_of(ct, i); nb = nb < NB1 ? nb : NB1 - 1;                                                    \
        int ks_ = 4 * (c) + wv;                                                                                 \
        const char* gp_ = W1T + ((size_t)nb * KS1 + ks_) * 1024;                                                \
        asm volatile("s_mov_b32 m0, %2\n\ts_nop 0\n\tglobal_load_lds_dwordx4 %0, %1"                            \
                     :: "v"(voff), "s"(gp_), "s"(lds0 + (unsigned)((ks_ * 7 + (i)) * 1024)) : "memory");        \
    }
#define ISSUE_X(c) LX(0, 4 * (c)) LX(1, 4 * (c)) LX(0, 4 * (c) + 1) LX(1, 4 * (c) + 1) LX(0, 4 * (c) + 2) LX(1, 4 * (c) + 2) LX(0, 4 * (c) + 3) LX(1, 4 * (c) + 3)
#define ISSUE_WA(c) LW(c, 0) LW(c, 1) LW(c, 2) LW(c, 3)
#define WAIT_X(c, n)                                                                                           \
    asm volatile("s_waitcnt vmcnt(" #n ")"                                                                      \
                 : "+v"(xf[0][4 * (c)]), "+v"(xf[0][4 * (c) + 1]), "+v"(xf[0][4 * (c) + 2]), "+v"(xf[0][4 * (c) + 3]), \
                   "+v"(xf[1][4 * (c)]), "+v"(xf[1][4 * (c) + 1]), "+v"(xf[1][4 * (c) + 2]), "+v"(xf[1][4 * (c) + 3])  \
                 :: "memory");
#define BARR() __builtin_amdgcn_s_barrier(); asm volatile("" ::: "memory");
#define CSV_LANDED() asm volatile("" : "+v"(csv[0]), "+v"(csv[1]), "+v"(csv[2]), "+v"(csv[3]), "+v"(csv[4]), "+v"(csv[5]), "+v"(csv[6]))
#define WAITL4(w) asm volatile("s_waitcnt lgkmcnt(0)" : "+v"(w[0]), "+v"(w[1]), "+v"(w[2]), "+v"(w[3]) :: "memory")
#define MM2(wf, wi, ks, i)                                                                                      \
    acc[0][i] = __builtin_amdgcn_mfma_f32_16x16x32_bf16(wf[wi], xf[0][ks], acc[0][i], 0, 0, 0);                 \
    acc[1][i] = __builtin_amdgcn_mfma_f32_16x16x32_bf16(wf[wi], xf[1][ks], acc[1][i], 0, 0, 0);
#define LDW1(dst, di, ks, i) { DSR(dst[di], lbase + ((ks) * 7168 / 57344) * 57344, ((ks) * 7168) % 57344 + (i) * 1024); }
#define STEPA(cur, nxt, ks, PRE, S0, S1, S2, S3)                                                                \
    {                                                                                                           \
        PRE                                                                                                     \
        WAITL4(cur);                                                                                            \
        MM2(cur, 0, ks, 0) SB(); if ((ks) < 19) LDW1(nxt, 0, (ks) + 1, 0) else LDW1(nxt, 0, 0, 4) S0 SB();      \
        MM2(cur, 1, ks, 1) SB(); if ((ks) < 19) LDW1(nxt, 1, (ks) + 1, 1) else LDW1(nxt, 1, 1, 4) S1 SB();      \
        MM2(cur, 2, ks, 2) SB(); if ((ks) < 19) LDW1(nxt, 2, (ks) + 1, 2) else LDW1(nxt, 2, 2, 4) S2 SB();      \
        if ((ks) < 19) LDW1(nxt, 3, (ks) + 1, 3) SB();                                                          \
        MM2(cur, 3, ks, 3)                                                                                      \
        if (L == 0) {                                                                                           \
            asum[0] = __builtin_amdgcn_mfma_f32_16x16x32_bf16(ones, xf[0][ks], asum[0], 0, 0, 0);               \
            asum[1] = __builtin_amdgcn_mfma_f32_16x16x32_bf16(ones, xf[1][ks], asum[1], 0, 0, 0);               \
            SB(); S3 SB();                                                                                      \
            asq[0] = __builtin_amdgcn_mfma_f32_16x16x32_bf16(xf[0][ks], xf[0][ks], asq[0], 0, 0, 0);            \
            asq[1] = __builtin_amdgcn_mfma_f32_16x16x32_bf16(xf[1][ks], xf[1][ks], asq[1], 0, 0, 0);            \
            SB();                                                                                               \
        } else { SB(); S3 SB(); }                                                                               \
    }
#define CHUNKA_XW(c, cn, PRE3)                                                                                  \
        STEPA(wA, wB, 4 * (c), , LX(0, 4 * (cn)), LX(1, 4 * (cn)), LX(0, 4 * (cn) + 1), LX(1, 4 * (cn) + 1))    \
        STEPA(wB, wA, 4 * (c) + 1, , LX(0, 4 * (cn) + 2), LX(1, 4 * (cn) + 2), LX(0, 4 * (cn) + 3), LX(1, 4 * (cn) + 3)) \
        STEPA(wA, wB, 4 * (c) + 2, , LW(cn, 0), LW(cn, 1), LW(cn, 2), LW(cn, 3))                                \
        STEPA(wB, wA, 4 * (c) + 3, PRE3, , , , )
#define CHUNKA_X(c, cn, PRE3)                                                                                   \
        STEPA(wA, wB, 4 * (c), , LX(0, 4 * (cn)), LX(1, 4 * (cn)), LX(0, 4 * (cn) + 1), )                       \
        STEPA(wB, wA, 4 * (c) + 1, , LX(1, 4 * (cn) + 1), LX(0, 4 * (cn) + 2), LX(1, 4 * (cn) + 2), )           \
        STEPA(wA, wB, 4 * (c) + 2, , LX(0, 4 * (cn) + 3), LX(1, 4 * (cn) + 3), , )                              \
        STEPA(wB, wA, 4 * (c) + 3, PRE3, , , , )
#define CHUNKA_0(c, PRE3)                                                                                       \
        STEPA(wA, wB, 4 * (c), , , , , )                                                                        \
        STEPA(wB, wA, 4 * (c) + 1, , , , , )                                                                    \
        STEPA(wA, wB, 4 * (c) + 2, , , , , )                                                                    \
        STEPA(wB, wA, 4 * (c) + 3, PRE3, , , , )
        const unsigned lbase = (unsigned)(size_t)(__attribute__((address_space(3))) char*)smem + lane * 16;
        f32x4 acc[2][7];
#pragma unroll
        for (int i = 0; i < 7; ++i) { acc[0][i] = (f32x4){0.f, 0.f, 0.f, 0.f}; acc[1][i] = (f32x4){0.f, 0.f, 0.f, 0.f}; }
        f32x4 asum[2] = {(f32x4){0.f, 0.f, 0.f, 0.f}, (f32x4){0.f, 0.f, 0.f, 0.f}};
        f32x4 asq[2] = {(f32x4){0.f, 0.f, 0.f, 0.f}, (f32x4){0.f, 0.f, 0.f, 0.f}};
        const bf16x8 ones = {0x3f80, 0x3f80, 0x3f80, 0x3f80, 0x3f80, 0x3f80, 0x3f80, 0x3f80};
        float mm[2], ri[2], dnv[2];
        auto row_stats = [&]() {
#pragma unroll
        for (int r = 0; r < 2; ++r) {
            float mean, var;
            if (L == 0) {
                const int tk = lane & 15;
                float dsel = (tk & 2) ? ((tk & 1) ? asq[r][3] : asq[r][2]) : ((tk & 1) ? asq[r][1] : asq[r][0]);
                float bq = rowsum4((tk >> 2) == q ? dsel : 0.f);
                mean = asum[r][0] * (1.f / D_);
                var = fmaxf(bq * (1.f / D_) - mean * mean, 0.f);
            } else {
                const float st = rowsum4(__uint_as_float(pst[r].x) + __uint_as_float(pst[r].z));
                const float vt = rowsum4(__uint_as_float(pst[r].y) + __uint_as_float(pst[r].w));
                mean = st * (1.f / D_);
                var = fmaxf(vt * (1.f / D_) - mean * mean, 0.f);
            }
            mm[r] = rbf(mean);
            dnv[r] = rbf(sqrtf(rbf(rbf(var) + 1e-5f)));
            ri[r] = __builtin_amdgcn_rcpf(dnv[r]);
        }
        };
        bf16x8 wA[4], wB[4];
        asm volatile("s_nop 4" : "+s"(xbase) :: "memory");
        if (L == 0) {
            ISSUE_X(0) ISSUE_WA(0) ISSUE_X(1) ISSUE_WA(1)
            WAIT_X(0, 12) BARR()
            CSV_LANDED();
            STAMP(ws, 2, 1);
            LDW1(wA, 0, 0, 0) LDW1(wA, 1, 0, 1) LDW1(wA, 2, 0, 2) LDW1(wA, 3, 0, 3)
            CHUNKA_XW(0, 2, WAIT_X(1, 12) BARR())
            CHUNKA_XW(1, 3, WAIT_X(2, 12) BARR())
            CHUNKA_XW(2, 4, WAIT_X(3, 12) BARR())
            STEPA(wA, wB, 12, , LW(0, 4), LW(0, 5), LW(0, 6), LW(1, 4))
            STEPA(wB, wA, 13, , LW(1, 5), LW(1, 6), LW(2, 4), LW(2, 5))
            STEPA(wA, wB, 14, , LW(2, 6), LW(3, 4), LW(3, 5), LW(3, 6))
            STEPA(wB, wA, 15, WAIT_X(4, 12) BARR(), LW(4, 4), LW(4, 5), LW(4, 6), )
            STEPA(wA, wB, 16, , loadA1(0);, loadA1(1);, loadA1(2);, loadA1(3);)
            STEPA(wB, wA, 17, , loadA1(4);, loadA1(5);, loadA1(6);, loadA1(7);)
            STEPA(wA, wB, 18, , loadA1(8);, loadA1(9);, loadA1(10);, loadA1(11);)
            STEPA(wB, wA, 19, VMWAIT(12); BARR(), loadA1(12);, loadA1(13);, loadA1(14);, loadA1(15);)
        } else {
            ISSUE_X(0) ISSUE_X(1)
            {
                const char* gb = ws + OFF_GR2 + (size_t)rb0 * 1024;
                unsigned vo = (lane & 15) * 64 + q * 16;
                GLOAD_X4(pst[0], vo, gb, 0);
                GLOAD_X4(pst[1], vo, gb, 1024);
            }
            LDW1(wA, 0, 0, 0) LDW1(wA, 1, 0, 1) LDW1(wA, 2, 0, 2) LDW1(wA, 3, 0, 3)
            WAIT_X(0, 10)
            CSV_LANDED();
            STAMP(ws, 2, 9);
            CHUNKA_X(0, 2, WAIT_X(1, 10))
            CHUNKA_X(1, 3, WAIT_X(2, 8))
            asm volatile("" : "+v"(pst[0]), "+v"(pst[1]));
            CHUNKA_X(2, 4, WAIT_X(3, 8))
            STEPA(wA, wB, 12, , , , , )
            STEPA(wB, wA, 13, , row_stats();, , , )
            STEPA(wA, wB, 14, , , , , )
            STEPA(wB, wA, 15, WAIT_X(4, 0), , , , )
            CHUNKA_0(4, )
        }
        STAMP(ws, 2, L * 8 + 2);
        if (L == 0) row_stats();
        SB();
        u32x2 hst[2][7];
        auto store_pair = [&](int r, int i) {
            const int nb = nb_of(ct, i);
            u32x4 s4 = {hst[r][i].x, hst[r][i].y, hst[r][i + 1].x, hst[r][i + 1].y};
            const int so = nb < NB1 ? hoff + ((rb0 + r) * KS2 + (nb >> 1)) * 1024 : 0x7ffffc00;
            store16_sc1(s4, rs, voff, so);
        };
#define PIN(x) asm volatile("" : "+v"(x))
#define MM1(wf, wi, r, ks, i) acc[r][i] = __builtin_amdgcn_mfma_f32_16x16x32_bf16(wf[wi], xf[r][ks], acc[r][i], 0, 0, 0); SB();
        float tt[56], zz[56];
        const float nm[2] = {-mm[0], -mm[1]};
        const float rzv[2] = {ri[0] * -1.44269504089f, ri[1] * -1.44269504089f};
        auto store_single = [&](int r) {
            const int nb = nb_of(ct, 6);
            __builtin_amdgcn_raw_buffer_store_b64(hst[r][6], rs, voff + (nb & 1) * 8, hoff + ((rb0 + r) * KS2 + (nb >> 1)) * 1024, 16);
        };
#define NAF(lo, hi) (((hi) < F0 ? F0 : (hi)) - ((lo) < F0 ? F0 : (lo)))
        bf16x8 wq[6];
        wq[0] = wA[0]; wq[1] = wA[1]; wq[2] = wA[2];
        LDW1(wq, 3, 3, 4) LDW1(wq, 4, 4, 4) LDW1(wq, 5, 5, 4)
        asm volatile("s_waitcnt lgkmcnt(5)" : "+v"(wq[0]) :: "memory");
        MM1(wq, 0, 0, 0, 4) tt[0] = fmaf(nm[0], csv[0][0], acc[0][0][0]); PIN(tt[0]); SB();
        MM1(wq, 0, 1, 0, 4) LDW1(wq, 0, 6, 4) loadA1(16); SB();
        asm volatile("s_waitcnt lgkmcnt(5)" : "+v"(wq[1]) :: "memory");
        MM1(wq, 1, 0, 1, 4) tt[1] = fmaf(nm[0], csv[0][1], acc[0][0][1]); PIN(tt[1]); SB();
        MM1(wq, 1, 1, 1, 4) LDW1(wq, 1, 7, 4) zz[0] = tt[0] * rzv[0]; PIN(zz[0]); loadA1(17); SB();
        asm volatile("s_waitcnt lgkmcnt(5)" : "+v"(wq[2]) :: "memory");
        MM1(wq, 2, 0, 2, 4) SB();
        MM1(wq, 2, 1, 2, 4) LDW1(wq, 2, 8, 4) tt[2] = fmaf(nm[0], csv[0][2], acc[0][0][2]); PIN(tt[2]); zz[0] = __builtin_amdgcn_exp2f(zz[0]); PIN(zz[0]); loadA1(18); SB();
        asm volatile("s_waitcnt lgkmcnt(5)" : "+v"(wq[3]) :: "memory");
        MM1(wq, 3, 0, 3, 4) zz[1] = tt[1] * rzv[0]; PIN(zz[1]); SB();
        MM1(wq, 3, 1, 3, 4) LDW1(wq, 3, 9, 4) tt[3] = fmaf(nm[0], csv[0][3], acc[0][0][3]); PIN(tt[3]); zz[1] = __builtin_amdgcn_exp2f(zz[1]); PIN(zz[1]); loadA1(19); SB();
        asm volatile("s_waitcnt lgkmcnt(5)" : "+v"(wq[4]) :: "memory");
        MM1(wq, 4, 0, 4, 4) zz[2] = tt[2] * rzv[0]; PIN(zz[2]); SB();
        MM1(wq, 4, 1, 4, 4) LDW1(wq, 4, 10, 4) zz[0] = fmaf(zz[0], dnv[0], dnv[0]); PIN(zz[0]); SB();
        asm volatile("s_waitcnt lgkmcnt(5)" : "+v"(wq[5]) :: "memory");
        MM1(wq, 5, 0, 5, 4) tt[4] = fmaf(nm[0], csv[1][0], acc[0][1][0]); PIN(tt[4]); zz[2] = __builtin_amdgcn_exp2f(zz[2]); PIN(zz[2]); zz[3] = tt[3] * rzv[0]; PIN(zz[3]); SB();
        MM1(wq, 5, 1, 5, 4) LDW1(wq, 5, 11, 4) zz[0] = __builtin_amdgcn_rcpf(zz[0]); PIN(zz[0]); zz[1] = fmaf(zz[1], dnv[0], dnv[0]); PIN(zz[1]); loadA1(20); SB();
        asm volatile("s_waitcnt lgkmcnt(5)" : "+v"(wq[0]) :: "memory");
        MM1(wq, 0, 0, 6, 4) tt[5] = fmaf(nm[0], csv[1][1], acc[0][1][1]); PIN(tt[5]); zz[3] = __builtin_amdgcn_exp2f(zz[3]); PIN(zz[3]); SB();
        MM1(wq, 0, 1, 6, 4) LDW1(wq, 0, 12, 4) zz[4] = tt[4] * rzv[0]; PIN(zz[4]); zz[1] = __builtin_amdgcn_rcpf(zz[1]); PIN(zz[1]); loadA1(21); SB();
        asm volatile("s_waitcnt lgkmcnt(5)" : "+v"(wq[1]) :: "memory");
        MM1(wq, 1, 0, 7, 4) zz[2] = fmaf(zz[2], dnv[0], dnv[0]); PIN(zz[2]); tt[0] = tt[0] * zz[0]; PIN(tt[0]); SB();
        MM1(wq, 1, 1, 7, 4) LDW1(wq, 1, 13, 4) tt[6] = fmaf(nm[0], csv[1][2], acc[0][1][2]); PIN(tt[6]); zz[4] = __builtin_amdgcn_exp2f(zz[4]); PIN(zz[4]); zz[5] = tt[5] * rzv[0]; PIN(zz[5]); loadA1(22); SB();
        asm volatile("s_waitcnt lgkmcnt(5)" : "+v"(wq[2]) :: "memory");
        MM1(wq, 2, 0, 8, 4) zz[2] = __builtin_amdgcn_rcpf(zz[2]); PIN(zz[2]); zz[3] = fmaf(zz[3], dnv[0], dnv[0]); PIN(zz[3]); tt[1] = tt[1] * zz[1]; PIN(tt[1]); SB();
        MM1(wq, 2, 1, 8, 4) LDW1(wq, 2, 14, 4) tt[7] = fmaf(nm[0], csv[1][3], acc[0][1][3]); PIN(tt[7]); zz[5] = __builtin_amdgcn_exp2f(zz[5]); PIN(zz[5]); zz[6] = tt[6] * rzv[0]; PIN(zz[6]); SB();
        asm volatile("s_waitcnt lgkmcnt(5)" : "+v"(wq[3]) :: "memory");
        MM1(wq, 3, 0, 9, 4) zz[3] = __builtin_amdgcn_rcpf(zz[3]); PIN(zz[3]); zz[4] = fmaf(zz[4], dnv[0], dnv[0]); PIN(zz[4]); SB();
        MM1(wq, 3, 1, 9, 4) LDW1(wq, 3, 15, 4) tt[2] = tt[2] * zz[2]; PIN(tt[2]); hst[0][0].x = pk2(tt[0], tt[1]); loadA1(23); SB();
        asm volatile("s_waitcnt lgkmcnt(5)" : "+v"(wq[4]) :: "memory");
        MM1(wq, 4, 0, 10, 4) tt[8] = fmaf(nm[0], csv[2][0], acc[0][2][0]); PIN(tt[8]); zz[6] = __builtin_amdgcn_exp2f(zz[6]); PIN(zz[6]); zz[7] = tt[7] * rzv[0]; PIN(zz[7]); SB();
        MM1(wq, 4, 1, 10, 4) LDW1(wq, 4, 16, 4) zz[4] = __builtin_amdgcn_rcpf(zz[4]); PIN(zz[4]); zz[5] = fmaf(zz[5], dnv[0], dnv[0]); PIN(zz[5]); tt[3] = tt[3] * zz[3]; PIN(tt[3]); loadA1(24); SB();
        asm volatile("s_waitcnt lgkmcnt(5)" : "+v"(wq[5]) :: "memory");
        MM1(wq, 5, 0, 11, 4) tt[9] = fmaf(nm[0], csv[2][1], acc[0][2][1]); PIN(tt[9]); zz[7] = __builtin_amdgcn_exp2f(zz[7]); PIN(zz[7]); zz[8] = tt[8] * rzv[0]; PIN(zz[8]); SB();
        MM1(wq, 5, 1, 11, 4) LDW1(wq, 5, 17, 4) zz[5] = __builtin_amdgcn_rcpf(zz[5]); PIN(zz[5]); zz[6] = fmaf(zz[6], dnv[0], dnv[0]); PIN(zz[6]); loadA1(25); SB();
        asm volatile("s_waitcnt lgkmcnt(5)" : "+v"(wq[0]) :: "memory");
        MM1(wq, 0, 0, 12, 4) tt[4] = tt[4] * zz[4]; PIN(tt[4]); hst[0][0].y = pk2(tt[2], tt[3]); SB();
        MM1(wq, 0, 1, 12, 4) LDW1(wq, 0, 18, 4) tt[10] = fmaf(nm[0], csv[2][2], acc[0][2][2]); PIN(tt[10]); zz[8] = __builtin_amdgcn_exp2f(zz[8]); PIN(zz[8]); zz[9] = tt[9] * rzv[0]; PIN(zz[9]); SB();
        asm volatile("s_waitcnt lgkmcnt(5)" : "+v"(wq[1]) :: "memory");
        MM1(wq, 1, 0, 13, 4) zz[6] = __builtin_amdgcn_rcpf(zz[6]); PIN(zz[6]); zz[7] = fmaf(zz[7], dnv[0], dnv[0]); PIN(zz[7]); tt[5] = tt[5] * zz[5]; PIN(tt[5]); SB();
        MM1(wq, 1, 1, 13, 4) LDW1(wq, 1, 19, 4) tt[11] = fmaf(nm[0], csv[2][3], acc[0][2][3]); PIN(tt[11]); zz[9] = __builtin_amdgcn_exp2f(zz[9]); PIN(zz[9]); zz[10] = tt[10] * rzv[0]; PIN(zz[10]); loadA1(26); SB();
        asm volatile("s_waitcnt lgkmcnt(5)" : "+v"(wq[2]) :: "memory");
        MM1(wq, 2, 0, 14, 4) zz[7] = __builtin_amdgcn_rcpf(zz[7]); PIN(zz[7]); zz[8] = fmaf(zz[8], dnv[0], dnv[0]); PIN(zz[8]); SB();
        MM1(wq, 2, 1, 14, 4) LDW1(wq, 2, 0, 5) tt[6] = tt[6] * zz[6]; PIN(tt[6]); hst[0][1].x = pk2(tt[4], tt[5]); loadA1(27); SB();
        asm volatile("s_waitcnt lgkmcnt(5)" : "+v"(wq[3]) :: "memory");
        MM1(wq, 3, 0, 15, 4) tt[12] = fmaf(nm[0], csv[3][0], acc[0][3][0]); PIN(tt[12]); zz[10] = __builtin_amdgcn_exp2f(zz[10]); PIN(zz[10]); zz[11] = tt[11] * rzv[0]; PIN(zz[11]); SB();
        MM1(wq, 3, 1, 15, 4) LDW1(wq, 3, 1, 5) zz[8] = __builtin_amdgcn_rcpf(zz[8]); PIN(zz[8]); zz[9] = fmaf(zz[9], dnv[0], dnv[0]); PIN(zz[9]); tt[7] = tt[7] * zz[7]; PIN(tt[7]); loadA1(28); SB();
        asm volatile("s_waitcnt lgkmcnt(5)" : "+v"(wq[4]) :: "memory");
        MM1(wq, 4, 0, 16, 4) tt[13] = fmaf(nm[0], csv[3][1], acc[0][3][1]); PIN(tt[13]); zz[11] = __builtin_amdgcn_exp2f(zz[11]); PIN(zz[11]); zz[12] = tt[12] * rzv[0]; PIN(zz[12]); SB();
        MM1(wq, 4, 1, 16, 4) LDW1(wq, 4, 2, 5) zz[9] = __builtin_amdgcn_rcpf(zz[9]); PIN(zz[9]); zz[10] = fmaf(zz[10], dnv[0], dnv[0]); PIN(zz[10]); tt[8] = tt[8] * zz[8]; PIN(tt[8]); SB();
        asm volatile("s_waitcnt lgkmcnt(5)" : "+v"(wq[5]) :: "memory");
        MM1(wq, 5, 0, 17, 4) hst[0][1].y = pk2(tt[6], tt[7]); store_pair(0, 0); SB();
        MM1(wq, 5, 1, 17, 4) LDW1(wq, 5, 3, 5) tt[14] = fmaf(nm[0], csv[3][2], acc[0][3][2]); PIN(tt[14]); zz[12] = __builtin_amdgcn_exp2f(zz[12]); PIN(zz[12]); zz[13] = tt[13] * rzv[0]; PIN(zz[13]); loadA1(29); SB();
        asm volatile("s_waitcnt lgkmcnt(5)" : "+v"(wq[0]) :: "memory");
        MM1(wq, 0, 0, 18, 4) zz[10] = __builtin_amdgcn_rcpf(zz[10]); PIN(zz[10]); zz[11] = fmaf(zz[11], dnv[0], dnv[0]); PIN(zz[11]); tt[9] = tt[9] * zz[9]; PIN(tt[9]); SB();
        MM1(wq, 0, 1, 18, 4) LDW1(wq, 0, 4, 5) tt[15] = fmaf(nm[0], csv[3][3], acc[0][3][3]); PIN(tt[15]); zz[13] = __builtin_amdgcn_exp2f(zz[13]); PIN(zz[13]); zz[14] = tt[14] * rzv[0]; PIN(zz[14]); loadA1(30); SB();
        asm volatile("s_waitcnt lgkmcnt(5)" : "+v"(wq[1]) :: "memory");
        MM1(wq, 1, 0, 19, 4) zz[11] = __builtin_amdgcn_rcpf(zz[11]); PIN(zz[11]); zz[12] = fmaf(zz[12], dnv[0], dnv[0]); PIN(zz[12]); SB();
        MM1(wq, 1, 1, 19, 4) LDW1(wq, 1, 5, 5) tt[10] = tt[10] * zz[10]; PIN(tt[10]); hst[0][2].x = pk2(tt[8], tt[9]); loadA1(31); SB();
        asm volatile("s_waitcnt lgkmcnt(5)" : "+v"(wq[2]) :: "memory");
        MM1(wq, 2, 0, 0, 5) tt[16] = fmaf(nm[1], csv[0][0], acc[1][0][0]); PIN(tt[16]); zz[14] = __builtin_amdgcn_exp2f(zz[14]); PIN(zz[14]); zz[15] = tt[15] * rzv[0]; PIN(zz[15]); SB();
        MM1(wq, 2, 1, 0, 5) LDW1(wq, 2, 6, 5) zz[12] = __builtin_amdgcn_rcpf(zz[12]); PIN(zz[12]); zz[13] = fmaf(zz[13], dnv[0], dnv[0]); PIN(zz[13]); tt[11] = tt[11] * zz[11]; PIN(tt[11]); SB();
        asm volatile("s_waitcnt lgkmcnt(5)" : "+v"(wq[3]) :: "memory");
        MM1(wq, 3, 0, 1, 5) tt[17] = fmaf(nm[1], csv[0][1], acc[1][0][1]); PIN(tt[17]); zz[15] = __builtin_amdgcn_exp2f(zz[15]); PIN(zz[15]); zz[16] = tt[16] * rzv[1]; PIN(zz[16]); SB();
        MM1(wq, 3, 1, 1, 5) LDW1(wq, 3, 7, 5) zz[13] = __builtin_amdgcn_rcpf(zz[13]); PIN(zz[13]); zz[14] = fmaf(zz[14], dnv[0], dnv[0]); PIN(zz[14]); loadA1(32); SB();
        asm volatile("s_waitcnt lgkmcnt(5)" : "+v"(wq[4]) :: "memory");
        MM1(wq, 4, 0, 2, 5) tt[12] = tt[12] * zz[12]; PIN(tt[12]); hst[0][2].y = pk2(tt[10], tt[11]); SB();
        MM1(wq, 4, 1, 2, 5) LDW1(wq, 4, 8, 5) tt[18] = fmaf(nm[1], csv[0][2], acc[1][0][2]); PIN(tt[18]); zz[16] = __builtin_amdgcn_exp2f(zz[16]); PIN(zz[16]); zz[17] = tt[17] * rzv[1]; PIN(zz[17]); loadA1(33); SB();
        asm volatile("s_waitcnt lgkmcnt(5)" : "+v"(wq[5]) :: "memory");
        MM1(wq, 5, 0, 3, 5) zz[14] = __builtin_amdgcn_rcpf(zz[14]); PIN(zz[14]); zz[15] = fmaf(zz[15], dnv[0], dnv[0]); PIN(zz[15]); tt[13] = tt[13] * zz[13]; PIN(tt[13]); SB();
        MM1(wq, 5, 1, 3, 5) LDW1(wq, 5, 9, 5) tt[19] = fmaf(nm[1], csv[0][3], acc[1][0][3]); PIN(tt[19]); zz[17] = __builtin_amdgcn_exp2f(zz[17]); PIN(zz[17]); zz[18] = tt[18] * rzv[1]; PIN(zz[18]); loadA1(34); SB();
        asm volatile("s_waitcnt lgkmcnt(5)" : "+v"(wq[0]) :: "memory");
        MM1(wq, 0, 0, 4, 5) zz[15] = __builtin_amdgcn_rcpf(zz[15]); PIN(zz[15]); zz[16] = fmaf(zz[16], dnv[1], dnv[1]); PIN(zz[16]); SB();
        MM1(wq, 0, 1, 4, 5) LDW1(wq, 0, 10, 5) tt[14] = tt[14] * zz[14]; PIN(tt[14]); hst[0][3].x = pk2(tt[12], tt[13]); SB();
        asm volatile("s_waitcnt lgkmcnt(5)" : "+v"(wq[1]) :: "memory");
        MM1(wq, 1, 0, 5, 5) tt[20] = fmaf(nm[1], csv[1][0], acc[1][1][0]); PIN(tt[20]); zz[18] = __builtin_amdgcn_exp2f(zz[18]); PIN(zz[18]); zz[19] = tt[19] * rzv[1]; PIN(zz[19]); SB();
        MM1(wq, 1, 1, 5, 5) LDW1(wq, 1, 11, 5) zz[16] = __builtin_amdgcn_rcpf(zz[16]); PIN(zz[16]); zz[17] = fmaf(zz[17], dnv[1], dnv[1]); PIN(zz[17]); tt[15] = tt[15] * zz[15]; PIN(tt[15]); loadA1(35); SB();
        asm volatile("s_waitcnt lgkmcnt(5)" : "+v"(wq[2]) :: "memory");
        MM1(wq, 2, 0, 6, 5) tt[21] = fmaf(nm[1], csv[1][1], acc[1][1][1]); PIN(tt[21]); zz[19] = __builtin_amdgcn_exp2f(zz[19]); PIN(zz[19]); zz[20] = tt[20] * rzv[1]; PIN(zz[20]); SB();
        MM1(wq, 2, 1, 6, 5) LDW1(wq, 2, 12, 5) zz[17] = __builtin_amdgcn_rcpf(zz[17]); PIN(zz[17]); zz[18] = fmaf(zz[18], dnv[1], dnv[1]); PIN(zz[18]); tt[16] = tt[16] * zz[16]; PIN(tt[16]); loadA1(36); SB();
        asm volatile("s_waitcnt lgkmcnt(5)" : "+v"(wq[3]) :: "memory");
        MM1(wq, 3, 0, 7, 5) hst[0][3].y = pk2(tt[14], tt[15]); store_pair(0, 2); SB();
        MM1(wq, 3, 1, 7, 5) LDW1(wq, 3, 13, 5) tt[22] = fmaf(nm[1], csv[1][2], acc[1][1][2]); PIN(tt[22]); zz[20] = __builtin_amdgcn_exp2f(zz[20]); PIN(zz[20]); zz[21] = tt[21] * rzv[1]; PIN(zz[21]); loadA1(37); SB();
        asm volatile("s_waitcnt lgkmcnt(5)" : "+v"(wq[4]) :: "memory");
        MM1(wq, 4, 0, 8, 5) zz[18] = __builtin_amdgcn_rcpf(zz[18]); PIN(zz[18]); zz[19] = fmaf(zz[19], dnv[1], dnv[1]); PIN(zz[19]); tt[17] = tt[17] * zz[17]; PIN(tt[17]); SB();
        MM1(wq, 4, 1, 8, 5) LDW1(wq, 4, 14, 5) tt[23] = fmaf(nm[1], csv[1][3], acc[1][1][3]); PIN(tt[23]); zz[21] = __builtin_amdgcn_exp2f(zz[21]); PIN(zz[21]); zz[22] = tt[22] * rzv[1]; PIN(zz[22]); SB();
        asm volatile("s_waitcnt lgkmcnt(5)" : "+v"(wq[5]) :: "memory");
        MM1(wq, 5, 0, 9, 5) zz[19] = __builtin_amdgcn_rcpf(zz[19]); PIN(zz[19]); zz[20] = fmaf(zz[20], dnv[1], dnv[1]); PIN(zz[20]); SB();
        MM1(wq, 5, 1, 9, 5) LDW1(wq, 5, 15, 5) tt[18] = tt[18] * zz[18]; PIN(tt[18]); hst[1][0].x = pk2(tt[16], tt[17]); loadA1(38); SB();
        asm volatile("s_waitcnt lgkmcnt(5)" : "+v"(wq[0]) :: "memory");
        MM1(wq, 0, 0, 10, 5) tt[24] = fmaf(nm[1], csv[2][0], acc[1][2][0]); PIN(tt[24]); zz[22] = __builtin_amdgcn_exp2f(zz[22]); PIN(zz[22]); zz[23] = tt[23] * rzv[1]; PIN(zz[23]); SB();
        MM1(wq, 0, 1, 10, 5) LDW1(wq, 0, 16, 5) zz[20] = __builtin_amdgcn_rcpf(zz[20]); PIN(zz[20]); zz[21] = fmaf(zz[21], dnv[1], dnv[1]); PIN(zz[21]); tt[19] = tt[19] * zz[19]; PIN(tt[19]); loadA1(39); SB();
        asm volatile("s_waitcnt lgkmcnt(5)" : "+v"(wq[1]) :: "memory");
        MM1(wq, 1, 0, 11, 5) tt[25] = fmaf(nm[1], csv[2][1], acc[1][2][1]); PIN(tt[25]); zz[23] = __builtin_amdgcn_exp2f(zz[23]); PIN(zz[23]); zz[24] = tt[24] * rzv[1]; PIN(zz[24]); SB();
        MM1(wq, 1, 1, 11, 5) LDW1(wq, 1, 17, 5) zz[21] = __builtin_amdgcn_rcpf(zz[21]); PIN(zz[21]); zz[22] = fmaf(zz[22], dnv[1], dnv[1]); PIN(zz[22]); loadA1(40); SB();
        asm volatile("s_waitcnt lgkmcnt(5)" : "+v"(wq[2]) :: "memory");
        MM1(wq, 2, 0, 12, 5) tt[20] = tt[20] * zz[20]; PIN(tt[20]); hst[1][0].y = pk2(tt[18], tt[19]); SB();
        MM1(wq, 2, 1, 12, 5) LDW1(wq, 2, 18, 5) tt[26] = fmaf(nm[1], csv[2][2], acc[1][2][2]); PIN(tt[26]); zz[24] = __builtin_amdgcn_exp2f(zz[24]); PIN(zz[24]); zz[25] = tt[25] * rzv[1]; PIN(zz[25]); loadA1(41); SB();
        asm volatile("s_waitcnt lgkmcnt(5)" : "+v"(wq[3]) :: "memory");
        MM1(wq, 3, 0, 13, 5) zz[22] = __builtin_amdgcn_rcpf(zz[22]); PIN(zz[22]); zz[23] = fmaf(zz[23], dnv[1], dnv[1]); PIN(zz[23]); tt[21] = tt[21] * zz[21]; PIN(tt[21]); SB();
        MM1(wq, 3, 1, 13, 5) LDW1(wq, 3, 19, 5) tt[27] = fmaf(nm[1], csv[2][3], acc[1][2][3]); PIN(tt[27]); zz[25] = __builtin_amdgcn_exp2f(zz[25]); PIN(zz[25]); zz[26] = tt[26] * rzv[1]; PIN(zz[26]); SB();
        asm volatile("s_waitcnt lgkmcnt(5)" : "+v"(wq[4]) :: "memory");
        MM1(wq, 4, 0, 14, 5) zz[23] = __builtin_amdgcn_rcpf(zz[23]); PIN(zz[23]); zz[24] = fmaf(zz[24], dnv[1], dnv[1]); PIN(zz[24]); SB();
        MM1(wq, 4, 1, 14, 5) LDW1(wq, 4, 0, 6) tt[22] = tt[22] * zz[22]; PIN(tt[22]); hst[1][1].x = pk2(tt[20], tt[21]); loadA1(42); SB();
        asm volatile("s_waitcnt lgkmcnt(5)" : "+v"(wq[5]) :: "memory");
        MM1(wq, 5, 0, 15, 5) tt[28] = fmaf(nm[1], csv[3][0], acc[1][3][0]); PIN(tt[28]); zz[26] = __builtin_amdgcn_exp2f(zz[26]); PIN(zz[26]); zz[27] = tt[27] * rzv[1]; PIN(zz[27]); SB();
        MM1(wq, 5, 1, 15, 5) LDW1(wq, 5, 1, 6) zz[24] = __builtin_amdgcn_rcpf(zz[24]); PIN(zz[24]); zz[25] = fmaf(zz[25], dnv[1], dnv[1]); PIN(zz[25]); tt[23] = tt[23] * zz[23]; PIN(tt[23]); loadA1(43); SB();
        asm volatile("s_waitcnt lgkmcnt(5)" : "+v"(wq[0]) :: "memory");
        MM1(wq, 0, 0, 16, 5) tt[29] = fmaf(nm[1], csv[3][1], acc[1][3][1]); PIN(tt[29]); zz[27] = __builtin_amdgcn_exp2f(zz[27]); PIN(zz[27]); zz[28] = tt[28] * rzv[1]; PIN(zz[28]); SB();
        MM1(wq, 0, 1, 16, 5) LDW1(wq, 0, 2, 6) zz[25] = __builtin_amdgcn_rcpf(zz[25]); PIN(zz[25]); zz[26] = fmaf(zz[26], dnv[1], dnv[1]); PIN(zz[26]); tt[24] = tt[24] * zz[24]; PIN(tt[24]); loadA1(44); SB();
        asm volatile("s_waitcnt lgkmcnt(5)" : "+v"(wq[1]) :: "memory");
        MM1(wq, 1, 0, 17, 5) hst[1][1].y = pk2(tt[22], tt[23]); store_pair(1, 0); SB();
        MM1(wq, 1, 1, 17, 5) LDW1(wq, 1, 3, 6) tt[30] = fmaf(nm[1], csv[3][2], acc[1][3][2]); PIN(tt[30]); zz[28] = __builtin_amdgcn_exp2f(zz[28]); PIN(zz[28]); zz[29] = tt[29] * rzv[1]; PIN(zz[29]); SB();
        asm volatile("s_waitcnt lgkmcnt(5)" : "+v"(wq[2]) :: "memory");
        MM1(wq, 2, 0, 18, 5) zz[26] = __builtin_amdgcn_rcpf(zz[26]); PIN(zz[26]); zz[27] = fmaf(zz[27], dnv[1], dnv[1]); PIN(zz[27]); tt[25] = tt[25] * zz[25]; PIN(tt[25]); SB();
        MM1(wq, 2, 1, 18, 5) LDW1(wq, 2, 4, 6) tt[31] = fmaf(nm[1], csv[3][3], acc[1][3][3]); PIN(tt[31]); zz[29] = __builtin_amdgcn_exp2f(zz[29]); PIN(zz[29]); zz[30] = tt[30] * rzv[1]; PIN(zz[30]); loadA1(45); SB();
        asm volatile("s_waitcnt lgkmcnt(5)" : "+v"(wq[3]) :: "memory");
        MM1(wq, 3, 0, 19, 5) zz[27] = __builtin_amdgcn_rcpf(zz[27]); PIN(zz[27]); zz[28] = fmaf(zz[28], dnv[1], dnv[1]); PIN(zz[28]); SB();
        MM1(wq, 3, 1, 19, 5) LDW1(wq, 3, 5, 6) tt[26] = tt[26] * zz[26]; PIN(tt[26]); hst[1][2].x = pk2(tt[24], tt[25]); loadA1(46); SB();
        asm volatile("s_waitcnt lgkmcnt(5)" : "+v"(wq[4]) :: "memory");
        MM1(wq, 4, 0, 0, 6) tt[32] = fmaf(nm[0], csv[4][0], acc[0][4][0]); PIN(tt[32]); zz[30] = __builtin_amdgcn_exp2f(zz[30]); PIN(zz[30]); zz[31] = tt[31] * rzv[1]; PIN(zz[31]); SB();
        MM1(wq, 4, 1, 0, 6) LDW1(wq, 4, 6, 6) zz[28] = __builtin_amdgcn_rcpf(zz[28]); PIN(zz[28]); zz[29] = fmaf(zz[29], dnv[1], dnv[1]); PIN(zz[29]); tt[27] = tt[27] * zz[27]; PIN(tt[27]); loadA1(47); SB();
        asm volatile("s_waitcnt lgkmcnt(5)" : "+v"(wq[5]) :: "memory");
        MM1(wq, 5, 0, 1, 6) tt[33] = fmaf(nm[0], csv[4][1], acc[0][4][1]); PIN(tt[33]); zz[31] = __builtin_amdgcn_exp2f(zz[31]); PIN(zz[31]); zz[32] = tt[32] * rzv[0]; PIN(zz[32]); SB();
        MM1(wq, 5, 1, 1, 6) LDW1(wq, 5, 7, 6) zz[29] = __builtin_amdgcn_rcpf(zz[29]); PIN(zz[29]); zz[30] = fmaf(zz[30], dnv[1], dnv[1]); PIN(zz[30]); SB();
        asm volatile("s_waitcnt lgkmcnt(5)" : "+v"(wq[0]) :: "memory");
        MM1(wq, 0, 0, 2, 6) tt[28] = tt[28] * zz[28]; PIN(tt[28]); hst[1][2].y = pk2(tt[26], tt[27]); SB();
        MM1(wq, 0, 1, 2, 6) LDW1(wq, 0, 8, 6) tt[34] = fmaf(nm[0], csv[4][2], acc[0][4][2]); PIN(tt[34]); zz[32] = __builtin_amdgcn_exp2f(zz[32]); PIN(zz[32]); zz[33] = tt[33] * rzv[0]; PIN(zz[33]); loadA1(48); SB();
        asm volatile("s_waitcnt lgkmcnt(5)" : "+v"(wq[1]) :: "memory");
        MM1(wq, 1, 0, 3, 6) zz[30] = __builtin_amdgcn_rcpf(zz[30]); PIN(zz[30]); zz[31] = fmaf(zz[31], dnv[1], dnv[1]); PIN(zz[31]); tt[29] = tt[29] * zz[29]; PIN(tt[29]); SB();
        MM1(wq, 1, 1, 3, 6) LDW1(wq, 1, 9, 6) tt[35] = fmaf(nm[0], csv[4][3], acc[0][4][3]); PIN(tt[35]); zz[33] = __builtin_amdgcn_exp2f(zz[33]); PIN(zz[33]); zz[34] = tt[34] * rzv[0]; PIN(zz[34]); loadA1(49); SB();
        asm volatile("s_waitcnt lgkmcnt(5)" : "+v"(wq[2]) :: "memory");
        MM1(wq, 2, 0, 4, 6) zz[31] = __builtin_amdgcn_rcpf(zz[31]); PIN(zz[31]); zz[32] = fmaf(zz[32], dnv[0], dnv[0]); PIN(zz[32]); SB();
        MM1(wq, 2, 1, 4, 6) LDW1(wq, 2, 10, 6) tt[30] = tt[30] * zz[30]; PIN(tt[30]); hst[1][3].x = pk2(tt[28], tt[29]); loadA1(50); SB();
        asm volatile("s_waitcnt lgkmcnt(5)" : "+v"(wq[3]) :: "memory");
        MM1(wq, 3, 0, 5, 6) tt[36] = fmaf(nm[1], csv[4][0], acc[1][4][0]); PIN(tt[36]); zz[34] = __builtin_amdgcn_exp2f(zz[34]); PIN(zz[34]); zz[35] = tt[35] * rzv[0]; PIN(zz[35]); SB();
        MM1(wq, 3, 1, 5, 6) LDW1(wq, 3, 11, 6) zz[32] = __builtin_amdgcn_rcpf(zz[32]); PIN(zz[32]); zz[33] = fmaf(zz[33], dnv[0], dnv[0]); PIN(zz[33]); tt[31] = tt[31] * zz[31]; PIN(tt[31]); SB();
        asm volatile("s_waitcnt lgkmcnt(5)" : "+v"(wq[4]) :: "memory");
        MM1(wq, 4, 0, 6, 6) tt[37] = fmaf(nm[1], csv[4][1], acc[1][4][1]); PIN(tt[37]); zz[35] = __builtin_amdgcn_exp2f(zz[35]); PIN(zz[35]); zz[36] = tt[36] * rzv[1]; PIN(zz[36]); SB();
        MM1(wq, 4, 1, 6, 6) LDW1(wq, 4, 12, 6) zz[33] = __builtin_amdgcn_rcpf(zz[33]); PIN(zz[33]); zz[34] = fmaf(zz[34], dnv[0], dnv[0]); PIN(zz[34]); tt[32] = tt[32] * zz[32]; PIN(tt[32]); loadA1(51); SB();
        asm volatile("s_waitcnt lgkmcnt(5)" : "+v"(wq[5]) :: "memory");
        MM1(wq, 5, 0, 7, 6) hst[1][3].y = pk2(tt[30], tt[31]); store_pair(1, 2); SB();
        MM1(wq, 5, 1, 7, 6) LDW1(wq, 5, 13, 6) tt[38] = fmaf(nm[1], csv[4][2], acc[1][4][2]); PIN(tt[38]); zz[36] = __builtin_amdgcn_exp2f(zz[36]); PIN(zz[36]); zz[37] = tt[37] * rzv[1]; PIN(zz[37]); loadA1(52); SB();
        asm volatile("s_waitcnt lgkmcnt(5)" : "+v"(wq[0]) :: "memory");
        MM1(wq, 0, 0, 8, 6) zz[34] = __builtin_amdgcn_rcpf(zz[34]); PIN(zz[34]); zz[35] = fmaf(zz[35], dnv[0], dnv[0]); PIN(zz[35]); tt[33] = tt[33] * zz[33]; PIN(tt[33]); SB();
        MM1(wq, 0, 1, 8, 6) LDW1(wq, 0, 14, 6) tt[39] = fmaf(nm[1], csv[4][3], acc[1][4][3]); PIN(tt[39]); zz[37] = __builtin_amdgcn_exp2f(zz[37]); PIN(zz[37]); zz[38] = tt[38] * rzv[1]; PIN(zz[38]); loadA1(53); SB();
        asm volatile("s_waitcnt lgkmcnt(5)" : "+v"(wq[1]) :: "memory");
        MM1(wq, 1, 0, 9, 6) zz[35] = __builtin_amdgcn_rcpf(zz[35]); PIN(zz[35]); zz[36] = fmaf(zz[36], dnv[1], dnv[1]); PIN(zz[36]); SB();
        MM1(wq, 1, 1, 9, 6) LDW1(wq, 1, 15, 6) tt[34] = tt[34] * zz[34]; PIN(tt[34]); hst[0][4].x = pk2(tt[32], tt[33]); SB();
        asm volatile("s_waitcnt lgkmcnt(5)" : "+v"(wq[2]) :: "memory");
        MM1(wq, 2, 0, 10, 6) tt[40] = fmaf(nm[0], csv[5][0], acc[0][5][0]); PIN(tt[40]); zz[38] = __builtin_amdgcn_exp2f(zz[38]); PIN(zz[38]); zz[39] = tt[39] * rzv[1]; PIN(zz[39]); SB();
        MM1(wq, 2, 1, 10, 6) LDW1(wq, 2, 16, 6) zz[36] = __builtin_amdgcn_rcpf(zz[36]); PIN(zz[36]); zz[37] = fmaf(zz[37], dnv[1], dnv[1]); PIN(zz[37]); tt[35] = tt[35] * zz[35]; PIN(tt[35]); loadA1(54); SB();
        asm volatile("s_waitcnt lgkmcnt(5)" : "+v"(wq[3]) :: "memory");
        MM1(wq, 3, 0, 11, 6) tt[41] = fmaf(nm[0], csv[5][1], acc[0][5][1]); PIN(tt[41]); zz[39] = __builtin_amdgcn_exp2f(zz[39]); PIN(zz[39]); zz[40] = tt[40] * rzv[0]; PIN(zz[40]); SB();
        MM1(wq, 3, 1, 11, 6) LDW1(wq, 3, 17, 6) zz[37] = __builtin_amdgcn_rcpf(zz[37]); PIN(zz[37]); zz[38] = fmaf(zz[38], dnv[1], dnv[1]); PIN(zz[38]); loadA1(55); SB();
        asm volatile("s_waitcnt lgkmcnt(5)" : "+v"(wq[4]) :: "memory");
        MM1(wq, 4, 0, 12, 6) tt[36] = tt[36] * zz[36]; PIN(tt[36]); hst[0][4].y = pk2(tt[34], tt[35]); SB();
        MM1(wq, 4, 1, 12, 6) LDW1(wq, 4, 18, 6) tt[42] = fmaf(nm[0], csv[5][2], acc[0][5][2]); PIN(tt[42]); zz[40] = __builtin_amdgcn_exp2f(zz[40]); PIN(zz[40]); zz[41] = tt[41] * rzv[0]; PIN(zz[41]); loadA1(56); SB();
        asm volatile("s_waitcnt lgkmcnt(5)" : "+v"(wq[5]) :: "memory");
        MM1(wq, 5, 0, 13, 6) zz[38] = __builtin_amdgcn_rcpf(zz[38]); PIN(zz[38]); zz[39] = fmaf(zz[39], dnv[1], dnv[1]); PIN(zz[39]); tt[37] = tt[37] * zz[37]; PIN(tt[37]); SB();
        MM1(wq, 5, 1, 13, 6) LDW1(wq, 5, 19, 6) tt[43] = fmaf(nm[0], csv[5][3], acc[0][5][3]); PIN(tt[43]); zz[41] = __builtin_amdgcn_exp2f(zz[41]); PIN(zz[41]); zz[42] = tt[42] * rzv[0]; PIN(zz[42]); SB();
        asm volatile("s_waitcnt lgkmcnt(5)" : "+v"(wq[0]) :: "memory");
        MM1(wq, 0, 0, 14, 6) zz[39] = __builtin_amdgcn_rcpf(zz[39]); PIN(zz[39]); zz[40] = fmaf(zz[40], dnv[0], dnv[0]); PIN(zz[40]); SB();
        MM1(wq, 0, 1, 14, 6) tt[38] = tt[38] * zz[38]; PIN(tt[38]); hst[1][4].x = pk2(tt[36], tt[37]); loadA1(57); SB();
        asm volatile("s_waitcnt lgkmcnt(4)" : "+v"(wq[1]) :: "memory");
        MM1(wq, 1, 0, 15, 6) tt[44] = fmaf(nm[1], csv[5][0], acc[1][5][0]); PIN(tt[44]); zz[42] = __builtin_amdgcn_exp2f(zz[42]); PIN(zz[42]); zz[43] = tt[43] * rzv[0]; PIN(zz[43]); SB();
        MM1(wq, 1, 1, 15, 6) zz[40] = __builtin_amdgcn_rcpf(zz[40]); PIN(zz[40]); zz[41] = fmaf(zz[41], dnv[0], dnv[0]); PIN(zz[41]); tt[39] = tt[39] * zz[39]; PIN(tt[39]); loadA1(58); SB();
        asm volatile("s_waitcnt lgkmcnt(3)" : "+v"(wq[2]) :: "memory");
        MM1(wq, 2, 0, 16, 6) tt[45] = fmaf(nm[1], csv[5][1], acc[1][5][1]); PIN(tt[45]); zz[43] = __builtin_amdgcn_exp2f(zz[43]); PIN(zz[43]); zz[44] = tt[44] * rzv[1]; PIN(zz[44]); asm volatile("s_waitcnt vmcnt(%0)" :: "n"(0 + NAF(52, 59)) : "memory"); publish_count(ws, L * 32 + rp); SB();
        MM1(wq, 2, 1, 16, 6) zz[41] = __builtin_amdgcn_rcpf(zz[41]); PIN(zz[41]); zz[42] = fmaf(zz[42], dnv[0], dnv[0]); PIN(zz[42]); loadA1(59); SB();
        asm volatile("s_waitcnt lgkmcnt(2)" : "+v"(wq[3]) :: "memory");
        MM1(wq, 3, 0, 17, 6) tt[40] = tt[40] * zz[40]; PIN(tt[40]); hst[1][4].y = pk2(tt[38], tt[39]); SB();
        MM1(wq, 3, 1, 17, 6) tt[46] = fmaf(nm[1], csv[5][2], acc[1][5][2]); PIN(tt[46]); zz[44] = __builtin_amdgcn_exp2f(zz[44]); PIN(zz[44]); zz[45] = tt[45] * rzv[1]; PIN(zz[45]); SB();
        asm volatile("s_waitcnt lgkmcnt(1)" : "+v"(wq[4]) :: "memory");
        MM1(wq, 4, 0, 18, 6) zz[42] = __builtin_amdgcn_rcpf(zz[42]); PIN(zz[42]); zz[43] = fmaf(zz[43], dnv[0], dnv[0]); PIN(zz[43]); tt[41] = tt[41] * zz[41]; PIN(tt[41]); SB();
        MM1(wq, 4, 1, 18, 6) tt[47] = fmaf(nm[1], csv[5][3], acc[1][5][3]); PIN(tt[47]); zz[45] = __builtin_amdgcn_exp2f(zz[45]); PIN(zz[45]); zz[46] = tt[46] * rzv[1]; PIN(zz[46]); SB();
        asm volatile("s_waitcnt lgkmcnt(0)" : "+v"(wq[5]) :: "memory");
        MM1(wq, 5, 0, 19, 6) zz[43] = __builtin_amdgcn_rcpf(zz[43]); PIN(zz[43]); zz[44] = fmaf(zz[44], dnv[1], dnv[1]); PIN(zz[44]); SB();
        MM1(wq, 5, 1, 19, 6) tt[42] = tt[42] * zz[42]; PIN(tt[42]); hst[0][5].x = pk2(tt[40], tt[41]); SB();
        STAMP(ws, 2, L * 8 + 3);
        tt[48] = fmaf(nm[0], csv[6][0], acc[0][6][0]); PIN(tt[48]);
        zz[46] = __builtin_amdgcn_exp2f(zz[46]); PIN(zz[46]);
        zz[47] = tt[47] * rzv[1]; PIN(zz[47]);
        zz[44] = __builtin_amdgcn_rcpf(zz[44]); PIN(zz[44]);
        zz[45] = fmaf(zz[45], dnv[1], dnv[1]); PIN(zz[45]);
        tt[43] = tt[43] * zz[43]; PIN(tt[43]);
        SB();
        tt[49] = fmaf(nm[0], csv[6][1], acc[0][6][1]); PIN(tt[49]);
        zz[47] = __builtin_amdgcn_exp2f(zz[47]); PIN(zz[47]);
        zz[48] = tt[48] * rzv[0]; PIN(zz[48]);
        zz[45] = __builtin_amdgcn_rcpf(zz[45]); PIN(zz[45]);
        zz[46] = fmaf(zz[46], dnv[1], dnv[1]); PIN(zz[46]);
        tt[44] = tt[44] * zz[44]; PIN(tt[44]);
        hst[0][5].y = pk2(tt[42], tt[43]);
        store_pair(0, 4);
        SB();
        tt[50] = fmaf(nm[0], csv[6][2], acc[0][6][2]); PIN(tt[50]);
        zz[48] = __builtin_amdgcn_exp2f(zz[48]); PIN(zz[48]);
        zz[49] = tt[49] * rzv[0]; PIN(zz[49]);
        zz[46] = __builtin_amdgcn_rcpf(zz[46]); PIN(zz[46]);
        zz[47] = fmaf(zz[47], dnv[1], dnv[1]); PIN(zz[47]);
        tt[45] = tt[45] * zz[45]; PIN(tt[45]);
        SB();
        tt[51] = fmaf(nm[0], csv[6][3], acc[0][6][3]); PIN(tt[51]);
        zz[49] = __builtin_amdgcn_exp2f(zz[49]); PIN(zz[49]);
        zz[50] = tt[50] * rzv[0]; PIN(zz[50]);
        zz[47] = __builtin_amdgcn_rcpf(zz[47]); PIN(zz[47]);
        zz[48] = fmaf(zz[48], dnv[0], dnv[0]); PIN(zz[48]);
        tt[46] = tt[46] * zz[46]; PIN(tt[46]);
        hst[1][5].x = pk2(tt[44], tt[45]);
        SB();
        tt[52] = fmaf(nm[1], csv[6][0], acc[1][6][0]); PIN(tt[52]);
        zz[50] = __builtin_amdgcn_exp2f(zz[50]); PIN(zz[50]);
        zz[51] = tt[51] * rzv[0]; PIN(zz[51]);
        zz[48] = __builtin_amdgcn_rcpf(zz[48]); PIN(zz[48]);
        zz[49] = fmaf(zz[49], dnv[0], dnv[0]); PIN(zz[49]);
        tt[47] = tt[47] * zz[47]; PIN(tt[47]);
        SB();
        tt[53] = fmaf(nm[1], csv[6][1], acc[1][6][1]); PIN(tt[53]);
        zz[51] = __builtin_amdgcn_exp2f(zz[51]); PIN(zz[51]);
        zz[52] = tt[52] * rzv[1]; PIN(zz[52]);
        zz[49] = __builtin_amdgcn_rcpf(zz[49]); PIN(zz[49]);
        zz[50] = fmaf(zz[50], dnv[0], dnv[0]); PIN(zz[50]);
        tt[48] = tt[48] * zz[48]; PIN(tt[48]);
        hst[1][5].y = pk2(tt[46], tt[47]);
        store_pair(1, 4);
        SB();
        tt[54] = fmaf(nm[1], csv[6][2], acc[1][6][2]); PIN(tt[54]);
        zz[52] = __builtin_amdgcn_exp2f(zz[52]); PIN(zz[52]);
        zz[53] = tt[53] * rzv[1]; PIN(zz[53]);
        zz[50] = __builtin_amdgcn_rcpf(zz[50]); PIN(zz[50]);
        zz[51] = fmaf(zz[51], dnv[0], dnv[0]); PIN(zz[51]);
        tt[49] = tt[49] * zz[49]; PIN(tt[49]);
        SB();
        tt[55] = fmaf(nm[1], csv[6][3], acc[1][6][3]); PIN(tt[55]);
        zz[53] = __builtin_amdgcn_exp2f(zz[53]); PIN(zz[53]);
        zz[54] = tt[54] * rzv[1]; PIN(zz[54]);
        zz[51] = __builtin_amdgcn_rcpf(zz[51]); PIN(zz[51]);
        zz[52] = fmaf(zz[52], dnv[1], dnv[1]); PIN(zz[52]);
        tt[50] = tt[50] * zz[50]; PIN(tt[50]);
        hst[0][6].x = pk2(tt[48], tt[49]);
        SB();
        zz[54] = __builtin_amdgcn_exp2f(zz[54]); PIN(zz[54]);
        zz[55] = tt[55] * rzv[1]; PIN(zz[55]);
        zz[52] = __builtin_amdgcn_rcpf(zz[52]); PIN(zz[52]);
        zz[53] = fmaf(zz[53], dnv[1], dnv[1]); PIN(zz[53]);
        tt[51] = tt[51] * zz[51]; PIN(tt[51]);
        SB();
        zz[55] = __builtin_amdgcn_exp2f(zz[55]); PIN(zz[55]);
        zz[53] = __builtin_amdgcn_rcpf(zz[53]); PIN(zz[53]);
        zz[54] = fmaf(zz[54], dnv[1], dnv[1]); PIN(zz[54]);
        tt[52] = tt[52] * zz[52]; PIN(tt[52]);
        hst[0][6].y = pk2(tt[50], tt[51]);
        store_single(0);
        SB();
        zz[54] = __builtin_amdgcn_rcpf(zz[54]); PIN(zz[54]);
        zz[55] = fmaf(zz[55], dnv[1], dnv[1]); PIN(zz[55]);
        tt[53] = tt[53] * zz[53]; PIN(tt[53]);
        SB();
        zz[55] = __builtin_amdgcn_rcpf(zz[55]); PIN(zz[55]);
        tt[54] = tt[54] * zz[54]; PIN(tt[54]);
        hst[1][6].x = pk2(tt[52], tt[53]);
        SB();
        tt[55] = tt[55] * zz[55]; PIN(tt[55]);
        SB();
        hst[1][6].y = pk2(tt[54], tt[55]);
        store_single(1);
        SB();
#pragma unroll
        for (int f = 60; f < 70; ++f) loadA1(f);
        SB();
        asm volatile("s_waitcnt vmcnt(%0)" :: "n"(F0 > 60 ? 70 - F0 : 10) : "memory");
    }
    publish_count(ws, L * 32 + 16 + rp);
    STAMP(ws, 2, L * 8 + 4);
    wait_count(ws, L * 32 + (rq >> 1), 16u);
    STAMP(ws, 2, L * 8 + 5);
    {
        int hoffc[4];
#pragma unroll
        for (int c = 0; c < 4; ++c) hoffc[c] = hoff + (rq * 4 + ((wv + c) & 3)) * (KS2 * 1024);
        u32x2 yv[5];
        if (L == 0) {
#pragma unroll
            for (int j = 0; j < 5; ++j) {
                const int db = db_of(cu, j);
                const int so = ((rq * 4 + wv) * KS1 + (db >> 1)) * 1024 + (db & 1) * 512;
                xres[j] = __builtin_amdgcn_raw_buffer_load_b64(rs, voff, (int)OFF_X1T + ((rq * 4 + wv) * KS1 + (db >> 1)) * 1024 + (db & 1) * 8, 0);
                yv[j] = __builtin_amdgcn_raw_buffer_load_b64(rs, lane * 8, (int)OFF_YT + so, 0);
            }
            SB();
        }
        u32x4 B[G2_PH][4];
        auto loadB = [&](int st, int i) {
#pragma unroll
            for (int c = 0; c < 4; ++c) B[st][c] = __builtin_amdgcn_raw_buffer_load_b128(rs, voff, hoffc[c] + kst[i] * 1024, 16);
        };
#pragma unroll
        for (int p = 0; p < G2_PH - 1; ++p) { loadB(p, p); SB(); }
        f32x4 acc[5][4];
        unsigned early = 0;
#pragma unroll
        for (int i = 0; i < 14; ++i) {
            const int st = i % G2_PH;
            if (i + G2_PH - 1 == 5) {
                const char* cb = ws + OFF_CNT + (size_t)(L * 32 + 16 + (rq >> 1)) * 256;
                asm volatile("s_nop 4\n\tglobal_load_dword %0, %1, %2 sc1" : "=v"(early) : "v"(0u), "s"(cb) : "memory");
            }
            if (i + G2_PH - 1 == 8) {
                gu32* c = (gu32*)(ws + OFF_CNT + (size_t)(L * 32 + 16 + (rq >> 1)) * 256);
                unsigned spins = 0;
                asm volatile("s_waitcnt vmcnt(12)" : "+v"(early) :: "memory");
                if (__builtin_amdgcn_readfirstlane(early) < 16u)
                while (__builtin_amdgcn_readfirstlane(__hip_atomic_load(c, __ATOMIC_RELAXED, __HIP_MEMORY_SCOPE_AGENT)) < 16u) {
                    if (++spins > SPIN_CAP) break;
                }
                __builtin_amdgcn_fence(__ATOMIC_ACQUIRE, "wavefront");
                SB();
            }
            if (i + G2_PH - 1 < 14) loadB((i + G2_PH - 1) % G2_PH, i + G2_PH - 1);
            SB();
            if ((i != 7 || wv != 3) && (i != 13 || wv != 2)) {
#pragma unroll
                for (int c = 0; c < 4; ++c)
#pragma unroll
                    for (int j = 0; j < 5; ++j) {
 if (i == 0) asm volatile("v_mfma_f32_16x16x32_bf16 %0, %1, %2, 0" : "=&v"(acc[j][c]) : "a"(A[i][j]), "v"(B[st][c]));
                        else if (i * 5 + j < G2_NAG) asm volatile("v_mfma_f32_16x16x32_bf16 %0, %1, %2, %0" : "+v"(acc[j][c]) : "a"(A[i][j]), "v"(B[st][c]));
                        else                asm volatile("s_nop 1\n\tv_mfma_f32_16x16x32_bf16 %0, %1, %2, %0" : "+v"(acc[j][c]) : "v"(A[i][j]), "v"(B[st][c]));
                    }
            }
            SB();
        }
        asm volatile("s_nop 15\n\ts_nop 15" ::: "memory");
        SB();
        STAMP(ws, 2, L * 8 + 6);
        f32x4* red = (f32x4*)(smem + G1_LDS);
        f32x4 o[5];
#pragma unroll
        for (int j = 0; j < 5; ++j) o[j] = acc[j][0];
        if (L == 0) {
#pragma unroll
            for (int t = 1; t < 4; ++t) {
                const int dst = (wv + t) & 3;
#pragma unroll
                for (int j = 0; j < 5; ++j) red[(dst * 5 + j) * 64 + lane] = acc[j][t];
                __syncthreads();
#pragma unroll
                for (int j = 0; j < 5; ++j) o[j] += red[(wv * 5 + j) * 64 + lane];
                if (t < 3) __syncthreads();
            }
        } else {
            f32x4* big = (f32x4*)smem;
#pragma unroll
            for (int t = 1; t < 4; ++t)
#pragma unroll
                for (int j = 0; j < 5; ++j) big[(((t - 1) * 4 + ((wv + t) & 3)) * 5 + j) * 64 + lane] = acc[j][t];
            __syncthreads();
#pragma unroll
            for (int t = 1; t < 4; ++t)
#pragma unroll
                for (int j = 0; j < 5; ++j) o[j] += big[(((t - 1) * 4 + wv) * 5 + j) * 64 + lane];
        }
        const int r = lane & 15;
        const int rb = rq * 4 + wv;
        float x2[5][4];
#pragma unroll
        for (int j = 0; j < 5; ++j) {
            unsigned p01 = pk2(o[j][0], o[j][1]), p23 = pk2(o[j][2], o[j][3]);
            unsigned s01 = pk2(bflo(xres[j].x) + bflo(p01), bfhi(xres[j].x) + bfhi(p01));
            unsigned s23 = pk2(bflo(xres[j].y) + bflo(p23), bfhi(xres[j].y) + bfhi(p23));
            if (L == 1) {
                u32x2 st = {s01, s23};
                *(u32x2*)(out + ((size_t)(rb * 16 + r) * D_ + db_of(cu, j) * 16 + 4 * q) * 2) = st;
            } else {
                x2[j][0] = bflo(s01); x2[j][1] = bfhi(s01); x2[j][2] = bflo(s23); x2[j][3] = bfhi(s23);
            }
        }
        if (L == 0) {
            float s = 0.f, m2 = 0.f;
#pragma unroll
            for (int j = 0; j < 5; ++j)
#pragma unroll
                for (int e = 0; e < 4; ++e) { s += x2[j][e]; m2 = fmaf(x2[j][e], x2[j][e], m2); }
            s = rowsum4(s); m2 = rowsum4(m2);
            gu64* gr = (gu64*)(ws + OFF_GR) + (size_t)rb * 128 + r;
            if (q == 0) {
                unsigned us = __float_as_uint(s), um = __float_as_uint(m2);
                if (us == 0xffffffffu) us = 0x7fc00000u;
                if (um == 0xffffffffu) um = 0x7fc00000u;
                __hip_atomic_store(gr + cu * 16, ((unsigned long long)um << 32) | us, __ATOMIC_RELAXED, __HIP_MEMORY_SCOPE_AGENT);
            }
            float yf[5][4];
#pragma unroll
            for (int j = 0; j < 5; ++j) { yf[j][0] = bflo(yv[j].x); yf[j][1] = bfhi(yv[j].x); yf[j][2] = bflo(yv[j].y); yf[j][3] = bfhi(yv[j].y); }
            unsigned long long g0, g1;
            for (unsigned spins = 0;; ++spins) {
                g0 = __hip_atomic_load(gr + 32 * q, __ATOMIC_RELAXED, __HIP_MEMORY_SCOPE_AGENT);
                g1 = __hip_atomic_load(gr + 32 * q + 16, __ATOMIC_RELAXED, __HIP_MEMORY_SCOPE_AGENT);
                bool ok = ((unsigned)g0 != 0xffffffffu) && ((unsigned)(g0 >> 32) != 0xffffffffu) &&
                          ((unsigned)g1 != 0xffffffffu) && ((unsigned)(g1 >> 32) != 0xffffffffu);
                if (__all(ok) || spins > SPIN_CAP) break;
                SPIN_SLEEP();
            }
            STAMP(ws, 2, 7);
            const float st = rowsum4(__uint_as_float((unsigned)g0) + __uint_as_float((unsigned)g1));
            const float vt = rowsum4(__uint_as_float((unsigned)(g0 >> 32)) + __uint_as_float((unsigned)(g1 >> 32)));
            const float mean = st * (1.f / D_);
            const float var = fmaxf(vt * (1.f / D_) - mean * mean, 0.f);
            const float m = rbf(mean);
            const float rinv = __builtin_amdgcn_rcpf(rbf(sqrtf(rbf(rbf(var) + 1e-5f))));
            const float nmr = -m * rinv;
            float s3 = 0.f, m23 = 0.f;
#pragma unroll
            for (int j = 0; j < 5; ++j) {
                float v4[4];
#pragma unroll
                for (int e = 0; e < 4; ++e) v4[e] = fmaf(fmaf(x2[j][e], rinv, nmr), yf[j][e], x2[j][e]);
                u32x2 x3 = {pk2(v4[0], v4[1]), pk2(v4[2], v4[3])};
                xres[j] = x3;
                if (j & 1) {
                    const int db = db_of(cu, j - 1);
                    u32x4 s4 = {xres[j - 1].x, xres[j - 1].y, x3.x, x3.y};
                    store16_sc1(s4, rs, voff, (int)OFF_X3T + (rb * KS1 + (db >> 1)) * 1024);
                } else if (j == 4) {
                    const int db = db_of(cu, 4);
                    __builtin_amdgcn_raw_buffer_store_b64(x3, rs, voff + (db & 1) * 8, (int)OFF_X3T + (rb * KS1 + (db >> 1)) * 1024, 16);
                }
                const float f0 = bflo(x3.x), f1 = bfhi(x3.x), f2 = bflo(x3.y), f3 = bfhi(x3.y);
                s3 += (f0 + f1) + (f2 + f3);
                m23 = fmaf(f0, f0, fmaf(f1, f1, fmaf(f2, f2, fmaf(f3, f3, m23))));
            }
            s3 = rowsum4(s3); m23 = rowsum4(m23);
            if (q == 0) {
                u32x2 pv = {__float_as_uint(s3), __float_as_uint(m23)};
                __builtin_amdgcn_raw_buffer_store_b64(pv, rs, r * 64 + cu * 8, (int)OFF_GR2 + rb * 1024, 16);
            }
            VMWAIT(0);
            publish_count(ws, 64 + (rq >> 1));
            wait_count(ws, 64 + rp, 16u);
            STAMP(ws, 2, 8);
        }
    }
}

__global__ __launch_bounds__(256, 1) void k2_ffn(char* __restrict__ ws, char* __restrict__ out) {
    extern __shared__ __attribute__((aligned(16))) char smem[];
    STAMP(ws, 2, 0);
    const int b = blockIdx.x;
    const int xcd = b & 7, idx = b >> 3;
    const int rp = 4 * (xcd >> 1) + (idx >> 3), ct = 8 * (xcd & 1) + (idx & 7);
    const int rq = 4 * xcd + (idx >> 3), cu = idx & 7;
    const int lane = threadIdx.x & 63;
    const int wv = __builtin_amdgcn_readfirstlane(threadIdx.x >> 6);
    const __amdgpu_buffer_rsrc_t rs = __builtin_amdgcn_make_buffer_rsrc((void*)ws, 0, (int)WS_USED, 0x00020000);
    u32x2 xres[5];
    SB();
    bf16x8 A[14][5];
    ffn_layer<0>(ws, out, smem, rs, rp, ct, rq, cu, lane, wv, xres, A);
    ffn_layer<1>(ws, out, smem, rs, rp, ct, rq, cu, lane, wv, xres, A);
}

extern "C" void kernel_launch(void* const* d_in, const int* in_sizes, int n_in,
                              void* d_out, int out_size, void* d_ws, size_t ws_size,
                              hipStream_t stream) {
    char* ws = (char*)d_ws;
    hipFuncSetAttribute((const void*)k1_conv, hipFuncAttributeMaxDynamicSharedMemorySize, K1_LDS);
    hipFuncSetAttribute((const void*)k2_ffn, hipFuncAttributeMaxDynamicSharedMemorySize, FFN_LDS);
    k0_prep<<<256, 896, 0, stream>>>((const unsigned*)d_in[0], (const unsigned*)d_in[1], (const unsigned*)d_in[2],
                                     (const u32x4*)d_in[3], (const u32x4*)d_in[4], (const u32x4*)d_in[7], (const u32x4*)d_in[8], ws);
    k1_conv<<<160 + 27 + 30, 512, K1_LDS, stream>>>((const unsigned short*)d_in[4], (const unsigned short*)d_in[5],
                                                    (const unsigned*)d_in[7], (const unsigned*)d_in[8], ws);
    k2_ffn<<<256, 256, FFN_LDS, stream>>>(ws, (char*)d_out);
}
```
